```python
import torch

def _softmax(x, axis=-1):
    return torch.softmax(x, dim=axis)
def _gelu(x):
    return torch.nn.functional.gelu(x, approximate="tanh")
def _silu(x):
    return torch.nn.functional.silu(x)
def _sigmoid(x):
    return torch.sigmoid(x)
def _layernorm(x, axis=-1, eps=1e-5):
    m = x.mean(dim=axis, keepdim=True)
    v = x.var(dim=axis, keepdim=True, unbiased=False)
    return (x - m) / torch.sqrt(v + eps)
def _scatter_add(tgt, idx, src, axis=0):
    return tgt.clone().index_add_(axis, idx, src)
def _segment_sum(src, seg, n):
    out = torch.zeros((n,) + src.shape[1:], dtype=src.dtype)
    return out.index_add_(0, seg, src)
def _pad(x, pw):
    flat = tuple(v for pair in reversed(pw) for v in pair)
    return torch.nn.functional.pad(x, flat)

kernel_name = 'gated_mix__sliding_block_cached__dense_activation_gelu__decode'

def setup_inputs():

    _w = (lambda a: (lambda t: t.to(torch.bfloat16) if t.is_floating_point() else t)(torch.as_tensor(a)))
    def _A_setup():
        B, Sq, D = 1, 1, 576
        Hq, Hkv, Sk, Dh = 32, 16, 32768, 128
        assert Hq % Hkv == 0, (Hq, Hkv)
        return {
            "x": _w((lambda *s: torch.randn(*s, dtype=torch.bfloat16) * 0.05)(B, Sq, D)),
            "k_cache": _w((lambda *s: torch.randn(*s, dtype=torch.bfloat16) * 3.2)(B, Hkv, Sk, Dh)),
            "v_cache": _w((lambda *s: torch.randn(*s, dtype=torch.bfloat16) * 0.2)(B, Hkv, Sk, Dh)),
            "wq": _w((lambda *s: torch.randn(*s, dtype=torch.bfloat16) * 3.2)(D, Hq * Dh) * (D ** -0.5)),
            "wo": _w((lambda *s: torch.randn(*s, dtype=torch.bfloat16) * 0.2)(Hq * Dh, D) * ((Hq * Dh) ** -0.5)),
        }
    _A_in = _A_setup()

    def _B_setup():
        B, S, D, F = 1, 1, 576, 4608
        return {
            "x": _w((lambda *s: torch.randn(*s, dtype=torch.bfloat16) * 0.05)(B, S, D)),
            "w1": _w((lambda *s: torch.randn(*s, dtype=torch.bfloat16) * 0.2)(D, F) * (D ** -0.5)),
            "b1": _w((lambda *s: torch.randn(*s, dtype=torch.bfloat16) * 0.2)(F) * 0.01),
            "w2": _w((lambda *s: torch.randn(*s, dtype=torch.bfloat16) * 0.2)(F, D) * (F ** -0.5)),
            "b2": _w((lambda *s: torch.randn(*s, dtype=torch.bfloat16) * 0.05)(D) * 0.01),
        }
    _B_in = _B_setup()

    return {
        "A_x": _A_in["x"],
        "A_k_cache": _A_in["k_cache"],
        "A_v_cache": _A_in["v_cache"],
        "A_wq": _A_in["wq"],
        "A_wo": _A_in["wo"],
        "B_x": _B_in["x"],
        "B_w1": _B_in["w1"],
        "B_b1": _B_in["b1"],
        "B_w2": _B_in["w2"],
        "B_b2": _B_in["b2"],
        "gate_w": _w((lambda *s: torch.randn(*s, dtype=torch.bfloat16) * 0.05)(576) * 16.0),
    }

def reference(A_x, A_k_cache, A_v_cache, A_wq, A_wo, B_x, B_w1, B_b1, B_w2, B_b2, gate_w):
    def _A_ref(x, k_cache, v_cache, wq, wo):
        B, Sq, D = x.shape
        Hq, Dh = 32, 128
        Hkv, Sk = k_cache.shape[1], k_cache.shape[2]
        W = min(4096, max(256, Sk // 4))
        rep = Hq // Hkv
        q = (lambda x, *d: x.permute(*d))((x @ wq).reshape(B, Sq, Hq, Dh), 0, 2, 1, 3)
        k = (lambda x, n, axis: torch.repeat_interleave(x, n, dim=axis))(k_cache[:, :, Sk - W:, :], rep, axis=1)
        v = (lambda x, n, axis: torch.repeat_interleave(x, n, dim=axis))(v_cache[:, :, Sk - W:, :], rep, axis=1)
        scores = torch.einsum("bhqd,bhkd->bhqk", q, k) * (Dh ** -0.5)
        p = _softmax(scores, axis=-1)
        o = torch.einsum("bhqk,bhkd->bhqd", p, v)
        return (lambda x, *d: x.permute(*d))(o, 0, 2, 1, 3).reshape(B, Sq, Hq * Dh) @ wo

    def _B_ref(x, w1, b1, w2, b2):
        h = torch.einsum("bsd,df->bsf", x, w1) + b1
        h = _gelu(h)
        return torch.einsum("bsf,fd->bsd", h, w2) + b2

    x = A_x
    g = _sigmoid((x * gate_w).sum(-1, keepdims=True))
    a = _A_ref(x, A_k_cache, A_v_cache, A_wq, A_wo)
    b = _B_ref(x, B_w1, B_b1, B_w2, B_b2)
    return g * a + (1 - g) * b
```

```cpp
#include <hip/hip_runtime.h>
#include <hip/hip_bf16.h>
#include <stdint.h>

typedef unsigned short u16;
typedef unsigned u32x4 __attribute__((ext_vector_type(4)));
using bf16x2 = __attribute__((ext_vector_type(2))) __bf16;

constexpr int D = 576, HQ = 32, HKV = 16, SK = 32768, DH = 128, W = 4096, F = 4608;
constexpr int NSPLIT = 16, KPS = W / NSPLIT;

__device__ __forceinline__ float bf2f(u16 v) { return __builtin_bit_cast(float, (unsigned)v << 16); }
__device__ __forceinline__ float lo(unsigned v) { return __builtin_bit_cast(float, v << 16); }
__device__ __forceinline__ float hi(unsigned v) { return __builtin_bit_cast(float, v & 0xffff0000u); }
__device__ __forceinline__ float rbf(float f) { return __bfloat162float(__float2bfloat16(f)); }

__global__ void k1(const u16* x, const u16* wq, const u16* w1, const u16* b1, float* q, float* act) {
  __shared__ float xs[D];
  for (int i = threadIdx.x; i < D; i += blockDim.x) xs[i] = bf2f(x[i]);
  __syncthreads();
  int c = blockIdx.x * blockDim.x + threadIdx.x;
  if (c < HQ * DH) {
    float a = 0;
    for (int k = 0; k < D; ++k) a += xs[k] * bf2f(wq[(size_t)k * (HQ * DH) + c]);
    q[c] = rbf(a);
  } else if (c < HQ * DH + F) {
    int f = c - HQ * DH;
    float a = 0;
    for (int k = 0; k < D; ++k) a += xs[k] * bf2f(w1[(size_t)k * F + f]);
    float h = rbf(rbf(a) + bf2f(b1[f]));
    float t = tanhf(0.7978845608028654f * (h + 0.044715f * h * h * h));
    act[f] = rbf(0.5f * h * (1.f + t));
  }
}

__global__ void k2(const u16* kc, const u16* vc, const float* q, float* pm, float* pl, float* po) {
  __shared__ float qs[2][DH];
  __shared__ float ps[2][KPS];
  __shared__ float red[2][256];
  int s = blockIdx.x, h = blockIdx.y, t = threadIdx.x;
  qs[t >> 7][t & 127] = q[h * 256 + t];
  __syncthreads();
  const u16* kr = kc + ((size_t)h * SK + (SK - W) + s * KPS + t) * DH;
  float s0 = 0, s1 = 0;
  for (int d = 0; d < DH; ++d) { float kv = bf2f(kr[d]); s0 += kv * qs[0][d]; s1 += kv * qs[1][d]; }
  s0 *= 0.08838834764831845f; s1 *= 0.08838834764831845f;
  red[0][t] = s0; red[1][t] = s1;
  __syncthreads();
  float m0 = -INFINITY, m1 = -INFINITY;
  for (int i = 0; i < 256; ++i) { m0 = fmaxf(m0, red[0][i]); m1 = fmaxf(m1, red[1][i]); }
  float p0 = expf(s0 - m0), p1 = expf(s1 - m1);
  ps[0][t] = p0; ps[1][t] = p1;
  __syncthreads();
  int hh = t >> 7, d = t & 127;
  float acc = 0, l = 0;
  const u16* vr = vc + ((size_t)h * SK + (SK - W) + s * KPS) * DH + d;
  for (int i = 0; i < KPS; ++i) { acc += ps[hh][i] * bf2f(vr[(size_t)i * DH]); l += ps[hh][i]; }
  int slot = (h * 2 + hh) * NSPLIT + s;
  po[(size_t)slot * DH + d] = acc;
  if (d == 0) { pm[slot] = hh ? m1 : m0; pl[slot] = l; }
}

__global__ void k3(const float* pm, const float* pl, const float* po, float* o) {
  int hq = blockIdx.x, d = threadIdx.x;
  float M = -INFINITY;
  for (int s = 0; s < NSPLIT; ++s) M = fmaxf(M, pm[hq * NSPLIT + s]);
  float L = 0, a = 0;
  for (int s = 0; s < NSPLIT; ++s) { float w = expf(pm[hq * NSPLIT + s] - M); L += w * pl[hq * NSPLIT + s]; a += w * po[(size_t)(hq * NSPLIT + s) * DH + d]; }
  o[hq * DH + d] = rbf(a / L);
}

__global__ void k4(const u16* x, const u16* gw, const float* o, const u16* wo, const float* act, const u16* w2, const u16* b2, __hip_bfloat16* out) {
  __shared__ float ra[16][64], rb[16][64];
  int c = blockIdx.x * 64 + (threadIdx.x & 63), g = threadIdx.x >> 6;
  float a = 0, b = 0;
  for (int r = g; r < HQ * DH; r += 16) a += o[r] * bf2f(wo[(size_t)r * D + c]);
  for (int r = g; r < F; r += 16) b += act[r] * bf2f(w2[(size_t)r * D + c]);
  ra[g][threadIdx.x & 63] = a; rb[g][threadIdx.x & 63] = b;
  __syncthreads();
  if (g == 0) {
    float gs = 0;
    for (int k = 0; k < D; ++k) gs += rbf(bf2f(x[k]) * bf2f(gw[k]));
    gs = rbf(gs);
    float gg = rbf(1.f / (1.f + expf(-gs)));
    a = 0; b = 0;
    for (int i = 0; i < 16; ++i) { a += ra[i][threadIdx.x]; b += rb[i][threadIdx.x]; }
    a = rbf(a); b = rbf(rbf(b) + bf2f(b2[c]));
    out[c] = __float2bfloat16(rbf(gg * a) + rbf(rbf(1.f - gg) * b));
  }
}

extern "C" void kernel_launch(void* const* d_in, const int* in_sizes, int n_in, void* d_out, int out_size, void* d_ws, size_t ws_size, hipStream_t stream) {
  const u16* x = (const u16*)d_in[0];
  const u16* kc = (const u16*)d_in[1];
  const u16* vc = (const u16*)d_in[2];
  const u16* wq = (const u16*)d_in[3];
  const u16* wo = (const u16*)d_in[4];
  const u16* w1 = (const u16*)d_in[6];
  const u16* b1 = (const u16*)d_in[7];
  const u16* w2 = (const u16*)d_in[8];
  const u16* b2 = (const u16*)d_in[9];
  const u16* gw = (const u16*)d_in[10];
  float* ws = (float*)d_ws;
  float* q = ws; float* act = ws + 4096; float* pm = ws + 16384; float* pl = pm + 512; float* po = ws + 32768; float* o = ws + 131072;
  k1<<<(4096 + 4608) / 64, 64, 0, stream>>>(x, wq, w1, b1, q, act);
  k2<<<dim3(NSPLIT, HKV), 256, 0, stream>>>(kc, vc, q, pm, pl, po);
  k3<<<HQ, 128, 0, stream>>>(pm, pl, po, o);
  k4<<<9, 1024, 0, stream>>>(x, gw, o, wo, act, w2, b2, (__hip_bfloat16*)d_out);
}
```

```cpp
#include <hip/hip_runtime.h>
#include <hip/hip_bf16.h>
#include <stdint.h>

typedef unsigned short u16;
typedef unsigned u32x4 __attribute__((ext_vector_type(4)));
typedef float f32x4 __attribute__((ext_vector_type(4)));
typedef float f32x2 __attribute__((ext_vector_type(2)));
typedef short s16x8 __attribute__((ext_vector_type(8)));

constexpr int DM = 576, HKV = 16, SK = 32768, DH = 128, WIN = 4096, FF = 4608, NQ = 4096;
constexpr int OOB = 0x40000000;

constexpr unsigned WS_EPOCH = 0;
constexpr unsigned WS_Q = 4096;
constexpr unsigned H_ROW = 1280, O_ROW = 1152, Y_ROW = 2304;
constexpr unsigned WS_H = WS_Q + 256 * 512;
constexpr unsigned WS_O = WS_H + 256 * H_ROW;
constexpr unsigned WS_Y = WS_O + 256 * O_ROW;
constexpr unsigned WS_END = WS_Y + 256 * Y_ROW;
constexpr unsigned SPIN_CAP = 1u << 18;
#define FAR_SLEEP_3 12
#define POL_QH_ST "sc1 nt"
#define POL_QH_LD "sc1 nt"
#define POL_O_ST "sc1"
#define POL_O_LD "sc1"
#define POL_Y_ST "sc1"
#define POL_Y_LD "sc1"
#define K0 4
#define K1 10
#define O_SLEEP 12
#define FIN_SLEEP 12

#define STAMP(k) do {} while (0)
#define PIN() __builtin_amdgcn_sched_barrier(0)

typedef u32x4 srd_t;
__device__ __forceinline__ srd_t mk(const void* p, unsigned bytes) {
  const unsigned long long a = (unsigned long long)p;
  return srd_t{(unsigned)a, (unsigned)(a >> 32), bytes, 0x00020000u};
}
#define LD4(dst, srd, voff, soff, pol) asm volatile("buffer_load_dwordx4 %0, %1, %2, %3 offen " pol : "=v"(dst) : "v"(voff), "s"(srd), "s"(soff))
#define LD4Z(dst, srd, voff, pol) asm volatile("buffer_load_dwordx4 %0, %1, %2, 0 offen " pol : "=v"(dst) : "v"(voff), "s"(srd))
#define LD1Z(dst, srd, voff, pol) asm volatile("buffer_load_dword %0, %1, %2, 0 offen " pol : "=v"(dst) : "v"(voff), "s"(srd))
#define LDHZ(dst, srd, voff, pol) asm volatile("buffer_load_ushort %0, %1, %2, 0 offen " pol : "=v"(dst) : "v"(voff), "s"(srd))
#define ST4Z(val, srd, voff, pol) asm volatile("buffer_store_dwordx4 %0, %1, %2, 0 offen " pol "\n\ts_nop 1" :: "v"(val), "v"(voff), "s"(srd) : "memory")
#define ST1Z(val, srd, voff, pol) asm volatile("buffer_store_dword %0, %1, %2, 0 offen " pol :: "v"(val), "v"(voff), "s"(srd) : "memory")
#define STHZ(val, srd, voff, pol) asm volatile("buffer_store_short %0, %1, %2, 0 offen " pol :: "v"(val), "v"(voff), "s"(srd) : "memory")
#define WAITV1(n, a) asm volatile("s_waitcnt vmcnt(" #n ")" : "+v"(a))
#define WAITV2(n, a, b) asm volatile("s_waitcnt vmcnt(" #n ")" : "+v"(a), "+v"(b))
#define WAITV3(n, a, b, c) asm volatile("s_waitcnt vmcnt(" #n ")" : "+v"(a), "+v"(b), "+v"(c))
#define WAITN(n) asm volatile("s_waitcnt vmcnt(%0)" :: "i"(n))
#define TIE(a) asm volatile("" : "+v"(a))
__device__ __forceinline__ float asf(unsigned u) { return __builtin_bit_cast(float, u); }
__device__ __forceinline__ unsigned asu(float f) { return __builtin_bit_cast(unsigned, f); }
__device__ __forceinline__ float blo(unsigned v) { return asf(v << 16); }
__device__ __forceinline__ float bhi(unsigned v) { return asf(v & 0xffff0000u); }
typedef __bf16 bf16x2_t __attribute__((ext_vector_type(2)));
__device__ __forceinline__ unsigned cvtpk(float lo, float hi) {
  return __builtin_bit_cast(unsigned, __builtin_convertvector(f32x2{lo, hi}, bf16x2_t));
}
__device__ __forceinline__ float rbf(float f) { return blo(cvtpk(f, f)); }
template <int CTRL> __device__ __forceinline__ float dpp(float x) {
  return asf(__builtin_amdgcn_mov_dpp((int)asu(x), CTRL, 0xf, 0xf, true));
}
constexpr int XOR1 = 0xB1, XOR2 = 0x4E, HMIR = 0x141, ROR8 = 0x128, ROR4 = 0x124;
__device__ __forceinline__ float xrow_sum(float x) {
  auto s = __builtin_amdgcn_permlane16_swap(asu(x), asu(x), false, false);
  x = asf(s[0]) + asf(s[1]);
  auto t = __builtin_amdgcn_permlane32_swap(asu(x), asu(x), false, false);
  return asf(t[0]) + asf(t[1]);
}
__device__ __forceinline__ float xrow_max(float x) {
  auto s = __builtin_amdgcn_permlane16_swap(asu(x), asu(x), false, false);
  x = fmaxf(asf(s[0]), asf(s[1]));
  auto t = __builtin_amdgcn_permlane32_swap(asu(x), asu(x), false, false);
  return fmaxf(asf(t[0]), asf(t[1]));
}
__device__ __forceinline__ float row_sum(float x) {
  x += dpp<XOR1>(x); x += dpp<XOR2>(x); x += dpp<HMIR>(x); x += dpp<ROR8>(x); return x;
}
__device__ __forceinline__ float row_max(float x) {
  x = fmaxf(x, dpp<XOR1>(x)); x = fmaxf(x, dpp<XOR2>(x)); x = fmaxf(x, dpp<HMIR>(x)); x = fmaxf(x, dpp<ROR8>(x)); return x;
}
__device__ __forceinline__ void fma8(float (&acc)[8], u32x4 w, float x) {
#pragma unroll
  for (int k = 0; k < 4; ++k) { acc[2 * k] = fmaf(x, blo(w[k]), acc[2 * k]); acc[2 * k + 1] = fmaf(x, bhi(w[k]), acc[2 * k + 1]); }
}
#define PUT_QUAD(r, voff, T, a, b, c, d, pol) PUT_PAIR(r, voff, T, asf(cvtpk(a, b)), asf(cvtpk(c, d)), pol)
#define PUT_PAIR(r, voff, T, a, b, pol) do { const float a_ = (a), b_ = (b); const int o_ = (voff); \
    const u32x4 v_ = {asu(a_), (T) ^ asu(b_), asu(b_), (T) ^ asu(a_)}; ST4Z(v_, r, o_, pol); } while (0)
__device__ __forceinline__ bool pair_ok(u32x4 v, unsigned T) { return ((v[1] ^ v[2]) == T) & ((v[3] ^ v[0]) == T); }
typedef unsigned long long u64;
#define BALLOT(c) __builtin_amdgcn_ballot_w64(c)
#define SETTLE1(r, o0, T, live, a, far_sleep, pol) \
  _Pragma("unroll 1") for (unsigned spins_ = 0; spins_ < SPIN_CAP; ++spins_) { \
    const u64 ma_ = BALLOT((live) & !pair_ok(a, T)); \
    if (ma_ == 0) break; \
    if (far_sleep && __popcll(ma_) > 16) __builtin_amdgcn_s_sleep(far_sleep); \
    u64 sv_; \
    asm volatile("s_mov_b64 %1, exec\n\ts_mov_b64 exec, %2\n\tbuffer_load_dwordx4 %0, %3, %4, 0 offen " pol "\n\ts_mov_b64 exec, %1\n\ts_waitcnt vmcnt(0)" \
                 : "+v"(a), "=&s"(sv_) : "s"(ma_), "v"(o0), "s"(r) : "memory"); }
#define SETTLE2(r, o0, o1, T, a, b, pol) SETTLE2F(r, o0, o1, T, a, b, 0, pol)
#define SETTLE2F(r, o0, o1, T, a, b, far_sleep, pol) \
  _Pragma("unroll 1") for (unsigned spins_ = 0; spins_ < SPIN_CAP; ++spins_) { \
    const u64 ma_ = BALLOT(!pair_ok(a, T)), mb_ = BALLOT(!pair_ok(b, T)); \
    if ((ma_ | mb_) == 0) break; \
    if (far_sleep && __popcll(ma_) + __popcll(mb_) > 32) __builtin_amdgcn_s_sleep(far_sleep); \
    u64 sv_; \
    asm volatile("s_mov_b64 %2, exec\n\ts_mov_b64 exec, %3\n\tbuffer_load_dwordx4 %0, %5, %7, 0 offen " pol "\n\t" \
                 "s_mov_b64 exec, %4\n\tbuffer_load_dwordx4 %1, %6, %7, 0 offen " pol "\n\ts_mov_b64 exec, %2\n\ts_waitcnt vmcnt(0)" \
                 : "+v"(a), "+v"(b), "=&s"(sv_) : "s"(ma_), "s"(mb_), "v"(o0), "v"(o1), "s"(r) : "memory"); }
#define SETTLE4(r, o0, o1, o2, o3, T, a, b, c, d, pol) \
  _Pragma("unroll 1") for (unsigned spins_ = 0; spins_ < SPIN_CAP; ++spins_) { \
    const u64 m0_ = BALLOT(!(pair_ok(a, T) & pair_ok(b, T))), m1_ = BALLOT(!(pair_ok(c, T) & pair_ok(d, T))); \
    if ((m0_ | m1_) == 0) break; \
    u64 sv_; \
    asm volatile("s_mov_b64 %4, exec\n\ts_mov_b64 exec, %5\n\tbuffer_load_dwordx4 %0, %7, %11, 0 offen " pol "\n\tbuffer_load_dwordx4 %1, %8, %11, 0 offen " pol "\n\t" \
                 "s_mov_b64 exec, %6\n\tbuffer_load_dwordx4 %2, %9, %11, 0 offen " pol "\n\tbuffer_load_dwordx4 %3, %10, %11, 0 offen " pol "\n\t" \
                 "s_mov_b64 exec, %4\n\ts_waitcnt vmcnt(0)" \
                 : "+v"(a), "+v"(b), "+v"(c), "+v"(d), "=&s"(sv_) : "s"(m0_), "s"(m1_), "v"(o0), "v"(o1), "v"(o2), "v"(o3), "s"(r) : "memory"); }
__device__ __forceinline__ float fold32(float a, float b) {
  auto s = __builtin_amdgcn_permlane32_swap(asu(a), asu(b), false, false);
  return asf(s[0]) + asf(s[1]);
}
__device__ __forceinline__ float fold16(float a, float b) {
  auto s = __builtin_amdgcn_permlane16_swap(asu(a), asu(b), false, false);
  return asf(s[0]) + asf(s[1]);
}
struct Lds {
  float xs[DM];
  float gp[72];
  float qred[4][64];
  float qh[2][256];
  float hb[7][292];
  float ob[4][256];
  float ml[4][2][2];
  float act[24];
  float ou[32];
  float fin[4][4];
};

__global__ void __launch_bounds__(256, 2)
fused_decode(const u16* __restrict__ x, const u16* __restrict__ kc, const u16* __restrict__ vc, const u16* __restrict__ wq,
             const u16* __restrict__ wo, const u16* __restrict__ w1, const u16* __restrict__ b1, const u16* __restrict__ w2,
             const u16* __restrict__ b2, const u16* __restrict__ gw, unsigned char* ws, u16* out) {
  __shared__ __attribute__((aligned(16))) Lds L;
  STAMP(0);
  const int b = blockIdx.x, t = threadIdx.x, l = t & 63, w = __builtin_amdgcn_readfirstlane(t >> 6);
  const int h = ((b & 7) << 1) | ((b >> 3) & 1), s = b >> 4, gid = h * 16 + s;
  const int cb = s & 3, rg = s >> 2;

  const srd_t rws = mk(ws, WS_END);
  const srd_t rx = mk(x, DM * 2), rgw = mk(gw, DM * 2);
  const srd_t rwq = mk(wq + (size_t)(144 * rg) * NQ + h * 256 + cb * 64, 143 * 8192 + 128);
  const size_t kvo = ((size_t)h * SK + (SK - WIN) + s * 256) * DH;
  const srd_t rk = mk(kc + kvo, 65536), rv = mk(vc + kvo, 65536);
  const srd_t rw1 = mk(w1 + (size_t)(36 * s) * FF + h * 288, 35 * 9216 + 576);
  const srd_t rw2 = mk(w2 + (size_t)gid * 18 * DM, 18 * 1152);
  const srd_t rwo = mk(wo + (size_t)gid * 16 * DM, 16 * 1152);
  const srd_t rb1 = mk(b1 + gid * 18, 36);

  const srd_t rb2 = mk(b2, DM * 2), rout = mk(out, DM * 2);
  unsigned epoch; u32x4 xv, gv;
  { const int z = WS_EPOCH; LD1Z(epoch, rws, z, "sc1"); }
  { const int o = t * 16; LD4Z(xv, rx, o, ""); LD4Z(gv, rgw, o, ""); }
  u32x4 aq[5];
  {
    const int vo = (8 * w + (l >> 3)) * 8192 + (l & 7) * 16;
#pragma unroll
    for (int j = 0; j < 5; ++j) LD4(aq[j], rwq, vo, j * 32 * 8192, "nt");
  }
  u32x4 a1[6];
  const int r7 = t / 36, c36 = t - r7 * 36;
  {
    const int vo = t < 252 ? r7 * 9216 + c36 * 16 : OOB;
#pragma unroll
    for (int j = 0; j < 6; ++j) LD4(a1[j], rw1, vo, j * 7 * 9216, "nt");
  }
  unsigned b1v;
  { const int o = ((t >> 4) * 4 + (t & 3)) * 2; LDHZ(b1v, rb1, o, ""); }
  u32x4 ak[16], av[16];
  const int kvoff = w * 1024 + ((l & 15) >> 2) * 256 + (l & 3) * 16 + (l >> 4) * 64;
  const int vvoff = w * 1024 + l * 16;
#pragma unroll
  for (int j = 0; j < K0; ++j) LD4(ak[j], rk, kvoff, j * 4096, "nt");
  PIN();

  WAITN(12 + K0); TIE(epoch); TIE(xv); TIE(gv);
  if (t < 72) {
    float g = 0.f, xf[8];
#pragma unroll
    for (int k = 0; k < 4; ++k) {
      xf[2 * k] = blo(xv[k]); xf[2 * k + 1] = bhi(xv[k]);
      const unsigned pr = cvtpk(xf[2 * k] * blo(gv[k]), xf[2 * k + 1] * bhi(gv[k]));
      g += blo(pr); g += bhi(pr);
    }
    *(f32x4*)&L.xs[8 * t] = f32x4{xf[0], xf[1], xf[2], xf[3]};
    *(f32x4*)&L.xs[8 * t + 4] = f32x4{xf[4], xf[5], xf[6], xf[7]};
    L.gp[t] = g;
  }
  if (t >= 250) L.act[t - 232] = 0.f;
  unsigned T = __builtin_amdgcn_readfirstlane(epoch) + 1u;
  T = T ? T : 1u;
  __syncthreads();
  STAMP(1);
  float gate, ngate;
  {
    float v = L.gp[l] + (l < 8 ? L.gp[64 + l] : 0.f);
    v = rbf(xrow_sum(row_sum(v)));
    gate = rbf(1.f / (1.f + __expf(-v)));
    ngate = rbf(1.f - gate);
  }
  float x1[6];
#pragma unroll
  for (int j = 0; j < 6; ++j) x1[j] = L.xs[min(36 * s + 7 * j + r7, DM - 1)];
  {
    float acc[8] = {0.f, 0.f, 0.f, 0.f, 0.f, 0.f, 0.f, 0.f};
    const int r0 = 144 * rg + 8 * w + (l >> 3);
    float xr[5];
#pragma unroll
    for (int j = 0; j < 5; ++j) xr[j] = L.xs[min(r0 + 32 * j, DM - 1)];
    WAITN(7 + K0); TIE(aq[0]); TIE(aq[1]); TIE(aq[2]); TIE(aq[3]); TIE(aq[4]);
#pragma unroll
    for (int j = 0; j < 5; ++j) fma8(acc, aq[j], xr[j]);
    float z[4], y[2];
#pragma unroll
    for (int k = 0; k < 4; ++k) z[k] = fold32(acc[k], acc[k + 4]);
#pragma unroll
    for (int k = 0; k < 2; ++k) { y[k] = fold16(z[k], z[k + 2]); y[k] += dpp<ROR8>(y[k]); }
    if ((l & 8) == 0) *(f32x2*)&L.qred[w][8 * (l & 7) + 2 * (l >> 4)] = f32x2{y[0], y[1]};
  }
  __syncthreads();
  {
    f32x2 a = *(const f32x2*)&L.qred[0][2 * (t & 31)];
#pragma unroll
    for (int i = 1; i < 4; ++i) a += *(const f32x2*)&L.qred[i][2 * (t & 31)];
    PUT_PAIR(rws, t < 32 ? WS_Q + gid * 512 + t * 16 : OOB, T, a[0], a[1], POL_QH_ST);
  }
#pragma unroll
  for (int j = K0; j < K1; ++j) LD4(ak[j], rk, kvoff, j * 4096, "nt");
  PIN();
  STAMP(2);
  {
    float acc[8] = {0.f, 0.f, 0.f, 0.f, 0.f, 0.f, 0.f, 0.f};
    WAITN(2 + K1); TIE(a1[0]);
#pragma unroll
    for (int j = 1; j < 6; ++j) TIE(a1[j]);
#pragma unroll
    for (int j = 0; j < 6; ++j) fma8(acc, a1[j], x1[j]);
    if (t < 252) {
      *(f32x4*)&L.hb[r7][8 * c36] = f32x4{acc[0], acc[1], acc[2], acc[3]};
      *(f32x4*)&L.hb[r7][8 * c36 + 4] = f32x4{acc[4], acc[5], acc[6], acc[7]};
    }
  }
  __syncthreads();
  {
    const int tt = min(t, 79), j = tt / 5, c0 = 18 * j + 4 * (tt - 5 * j);
    f32x2 a = *(const f32x2*)&L.hb[0][c0], c = *(const f32x2*)&L.hb[0][c0 + 2];
#pragma unroll
    for (int i = 1; i < 7; ++i) { a += *(const f32x2*)&L.hb[i][c0]; c += *(const f32x2*)&L.hb[i][c0 + 2]; }
    PUT_QUAD(rws, t < 80 ? WS_H + gid * H_ROW + t * 16 : OOB, T, a[0], a[1], c[0], c[1], POL_QH_ST);
  }
#pragma unroll
  for (int j = K1; j < 16; ++j) LD4(ak[j], rk, kvoff, j * 4096, "nt");
  const int qo0 = WS_Q + (h * 16 + (t >> 7) * 8 + ((t & 127) >> 5)) * 512 + (t & 31) * 16, qo1 = qo0 + 4 * 512;
  u32x4 pa, pb, ph;
  LD4Z(pa, rws, qo0, POL_QH_LD); LD4Z(pb, rws, qo1, POL_QH_LD);
#pragma unroll
  for (int j = 0; j < 12; ++j) LD4(av[j], rv, vvoff, j * 4096, "nt");
  const int ho = t < 80 ? WS_H + (h * 16 + (t & 15)) * H_ROW + (s * 5 + (t >> 4)) * 16 : OOB;
  PIN();
  STAMP(3);
  WAITV2(12, pa, pb); TIE(b1v);
#pragma unroll
  for (int j = 0; j < 16; ++j) TIE(ak[j]);
  SETTLE2(rws, qo0, qo1, T, pa, pb, POL_QH_LD);
  *(f32x2*)&L.qh[t >> 7][2 * (t & 127)] = f32x2{asf(pa[0]) + asf(pb[0]), asf(pa[2]) + asf(pb[2])};
  __syncthreads();
  STAMP(4);
  u32x4 qf;
  {
    const int o = ((l >> 3) & 1) * 128 + ((l & 3) + 4 * (l >> 4)) * 8;
    const f32x4 a0 = *(const f32x4*)&L.qh[0][o] + *(const f32x4*)&L.qh[1][o];
    const f32x4 a1_ = *(const f32x4*)&L.qh[0][o + 4] + *(const f32x4*)&L.qh[1][o + 4];
    qf = u32x4{cvtpk(a0[0], a0[1]), cvtpk(a0[2], a0[3]), cvtpk(a1_[0], a1_[1]), cvtpk(a1_[2], a1_[3])};
  }
  constexpr float C2 = 0.08838834764831845f * 1.4426950408889634f;
  u32x4 a2[5], ao[4], a2x[3], aox[2];
  const int rho = l >> 4;
  const int wvo = rho * 1152 + w * 256 + (l & 15) * 16;
  const int x2o = w == 3 ? (l >> 3) * 1152 + 1024 + (l & 7) * 16 : OOB;
  float sc[16];
  {
    f32x4 d[16];
#pragma unroll
    for (int j = 0; j < 16; ++j)
      d[j] = __builtin_amdgcn_mfma_f32_16x16x32_bf16(__builtin_bit_cast(s16x8, ak[j]), __builtin_bit_cast(s16x8, qf), f32x4{0.f, 0.f, 0.f, 0.f}, 0, 0, 0);
    PIN();
#pragma unroll
    for (int j = 0; j < 16; ++j) sc[j] = (dpp<0x00>(d[j][0]) + dpp<0x55>(d[j][1])) + (dpp<0xAA>(d[j][2]) + dpp<0xFF>(d[j][3]));
  }
  float mx = -INFINITY;
#pragma unroll
  for (int j = 0; j < 16; ++j) mx = fmaxf(mx, sc[j]);
  mx = xrow_max(mx) * C2;
  float lsum = 0.f;
  unsigned pk[16];
#pragma unroll
  for (int j = 0; j < 16; ++j) { const float p = __builtin_amdgcn_exp2f(fmaf(sc[j], C2, -mx)); lsum += p; pk[j] = cvtpk(p, p); }
  lsum = xrow_sum(lsum);
  u32x4 msk;
  {
    const unsigned half = (l & 1) ? 0xffff0000u : 0x0000ffffu;
#pragma unroll
    for (int k = 0; k < 4; ++k) msk[k] = (((l & 7) >> 1) == k) ? half : 0u;
  }
  PIN();
  STAMP(5);
  f32x4 oacc = {0.f, 0.f, 0.f, 0.f};
#define PV_STEP(j, n) { const u32x4 pf = {pk[j] & msk[0], pk[j] & msk[1], pk[j] & msk[2], pk[j] & msk[3]}; WAITV1(n, av[j]); \
    oacc = __builtin_amdgcn_mfma_f32_16x16x32_bf16(__builtin_bit_cast(s16x8, pf), __builtin_bit_cast(s16x8, av[j]), oacc, 0, 0, 0); PIN(); }
  LD4(av[12], rv, vvoff, 12 * 4096, "nt"); LD4(av[13], rv, vvoff, 13 * 4096, "nt");
  LD4(av[14], rv, vvoff, 14 * 4096, "nt"); LD4(av[15], rv, vvoff, 15 * 4096, "nt");
  PV_STEP(0, 15)  LD4Z(ph, rws, ho, POL_QH_LD);
  PV_STEP(1, 15)  LD4(a2[0], rw2, wvo, 0, "nt");
  PV_STEP(2, 15)  LD4(a2[1], rw2, wvo, 4608, "nt");
  PV_STEP(3, 15)  LD4(a2[2], rw2, wvo, 2 * 4608, "nt");
  PV_STEP(4, 15)  LD4(a2[3], rw2, wvo, 3 * 4608, "nt");
  PV_STEP(5, 15)  LD4(a2[4], rw2, wvo, 4 * 4608, "nt");
  PV_STEP(6, 15)  LD4(a2x[0], rw2, x2o, 0, "nt");
  PV_STEP(7, 15)  LD4(a2x[1], rw2, x2o, 9216, "nt");
  PV_STEP(8, 15)  LD4(a2x[2], rw2, x2o, 2 * 9216, "nt");
  PV_STEP(9, 15)  LD4(ao[0], rwo, wvo, 0, "nt");
  PV_STEP(10, 15) LD4(ao[1], rwo, wvo, 4608, "nt");
  PV_STEP(11, 15) LD4(ao[2], rwo, wvo, 2 * 4608, "nt");
  PV_STEP(12, 15) LD4(ao[3], rwo, wvo, 3 * 4608, "nt");
  PV_STEP(13, 15) LD4(aox[0], rwo, x2o, 0, "nt");
  PV_STEP(14, 15) LD4(aox[1], rwo, x2o, 9216, "nt");
  PV_STEP(15, 15)
  *(f32x4*)&L.ob[w][(l >> 5) * 128 + (l & 15) * 8 + ((l >> 4) & 1) * 4] = oacc;
  if ((l & 55) == 0) *(f32x2*)&L.ml[w][l >> 3][0] = f32x2{mx, lsum};
  STAMP(6);
  __syncthreads();
  {
    const int hh = t < 64 ? t >> 5 : t & 1, oi = 4 * l;
    const f32x2 q0 = *(const f32x2*)&L.ml[0][hh][0], q1 = *(const f32x2*)&L.ml[1][hh][0], q2 = *(const f32x2*)&L.ml[2][hh][0], q3 = *(const f32x2*)&L.ml[3][hh][0];
    const f32x4 o0 = *(const f32x4*)&L.ob[0][oi], o1 = *(const f32x4*)&L.ob[1][oi], o2 = *(const f32x4*)&L.ob[2][oi], o3 = *(const f32x4*)&L.ob[3][oi];
    const float M = fmaxf(fmaxf(q0[0], q1[0]), fmaxf(q2[0], q3[0]));
    const float e0 = __builtin_amdgcn_exp2f(q0[0] - M), e1 = __builtin_amdgcn_exp2f(q1[0] - M), e2 = __builtin_amdgcn_exp2f(q2[0] - M), e3 = __builtin_amdgcn_exp2f(q3[0] - M);
    const f32x4 v = e0 * o0 + e1 * o1 + e2 * o2 + e3 * o3;
    const float ls = e0 * q0[1] + e1 * q1[1] + e2 * q2[1] + e3 * q3[1];
    const bool iso = t < 64;
    PUT_PAIR(rws, t < 66 ? WS_O + gid * O_ROW + t * 16 : OOB, T, iso ? asf(cvtpk(v[0], v[1])) : M, iso ? asf(cvtpk(v[2], v[3])) : ls, POL_O_ST);
  }
  PIN();
  STAMP(7);
  const int fin = (b & 7) * 18 + (b >> 3);
  const int ocol = (fin < 128 ? (fin >> 5) * 128 + (fin & 15) * 8 + ((fin >> 4) & 1) * 4 : 512 + (fin & 7) * 8 + ((fin >> 3) & 1) * 4) + (t & 3);
  unsigned b2v;
  { const int o = (b >> 3) < 18 ? ocol * 2 : OOB; LDHZ(b2v, rb2, o, ""); }
  WAITV1(16, ph);
  SETTLE1(rws, ho, T, t < 80, ph, 0, POL_QH_LD);
  {
    const float s0 = row_sum(blo(ph[0])), s1 = row_sum(bhi(ph[0])), s2 = row_sum(blo(ph[2])), s3 = row_sum(bhi(ph[2]));
    const float sv = (t & 2) ? ((t & 1) ? s3 : s2) : ((t & 1) ? s1 : s0);
    const float hv = rbf(rbf(sv) + blo(b1v));
    const float z = 0.7978845608028654f * (hv + 0.044715f * hv * hv * hv);
    const float th = 1.f - 2.f / (1.f + __expf(2.f * z));
    const int f = 4 * (t >> 4) + (t & 15);
    if ((t & 15) < 4 && f < 18) L.act[f] = ngate * rbf(0.5f * hv * (1.f + th));
  }
  __syncthreads();
  STAMP(12);
  float y[8] = {0.f, 0.f, 0.f, 0.f, 0.f, 0.f, 0.f, 0.f}, yx[8] = {0.f, 0.f, 0.f, 0.f, 0.f, 0.f, 0.f, 0.f};
  {
    float u[5], ux[3];
#pragma unroll
    for (int j = 0; j < 5; ++j) u[j] = L.act[4 * j + rho];
#pragma unroll
    for (int j = 0; j < 3; ++j) ux[j] = L.act[8 * j + (l >> 3)];
    WAITV1(8, a2[0]);
#pragma unroll
    for (int j = 1; j < 5; ++j) TIE(a2[j]);
#pragma unroll
    for (int j = 0; j < 3; ++j) TIE(a2x[j]);
#pragma unroll
    for (int j = 0; j < 5; ++j) fma8(y, a2[j], u[j]);
    if (w == 3) {
#pragma unroll
      for (int j = 0; j < 3; ++j) fma8(yx, a2x[j], ux[j]);
    }
#pragma unroll
    for (int k = 0; k < 8; ++k) { TIE(y[k]); TIE(yx[k]); }
  }
  PIN();
  STAMP(13);
  {
    const int ob_ = w == 0 ? WS_O + (h * 16 + (l >> 2)) * O_ROW : OOB;
    const int oo = ob_ + s * 64 + (l & 3) * 16, om = ob_ + 1024 + (s >> 3) * 16;
    const int orow = 4 * (l & 3) + rho;
    int oslot = (l & 4) ? 16 + (orow & 7) * 2 + (orow >> 3) : rho * 4 + (l & 3);
    int yo = (l & 16) == 0 ? WS_Y + b * Y_ROW + (32 * w + 16 * (l >> 5) + (l & 15)) * 16 : OOB;
    int yxo = (l & 24) == 0 ? WS_Y + b * Y_ROW + (128 + 8 * (l >> 5) + (l & 7)) * 16 : OOB;
    TIE(oslot); TIE(yo); TIE(yxo);
    u32x4 p0, m0;
    LD4Z(p0, rws, oo, POL_O_LD); LD4Z(m0, rws, om, POL_O_LD);
    WAITV1(2, ao[0]); TIE(ao[1]); TIE(ao[2]); TIE(ao[3]); TIE(aox[0]); TIE(aox[1]); TIE(b2v);
    float wf[4][8], wx[2][8];
#pragma unroll
    for (int j = 0; j < 4; ++j)
#pragma unroll
      for (int k = 0; k < 4; ++k) { wf[j][2 * k] = blo(ao[j][k]); wf[j][2 * k + 1] = bhi(ao[j][k]); TIE(wf[j][2 * k]); TIE(wf[j][2 * k + 1]); }
#pragma unroll
    for (int j = 0; j < 2; ++j)
#pragma unroll
      for (int k = 0; k < 4; ++k) { wx[j][2 * k] = blo(aox[j][k]); wx[j][2 * k + 1] = bhi(aox[j][k]); TIE(wx[j][2 * k]); TIE(wx[j][2 * k + 1]); }
    PIN();
    STAMP(14);
    WAITV2(0, p0, m0);
    STAMP(15);
    if (w == 0) {
      SETTLE2(rws, oo, om, T, p0, m0, POL_O_LD);
      STAMP(8);
      float M = asf(m0[0]);
      M = fmaxf(M, dpp<ROR4>(M)); M = fmaxf(M, dpp<ROR8>(M)); M = xrow_max(M);
      const float e = __builtin_amdgcn_exp2f(asf(m0[0]) - M);
      float Ls = e * asf(m0[2]);
      float v = fold16(fold32(e * blo(p0[0]), e * blo(p0[2])), fold32(e * bhi(p0[0]), e * bhi(p0[2])));
      Ls += dpp<ROR4>(Ls); v += dpp<ROR4>(v);
      Ls += dpp<ROR8>(Ls); v += dpp<ROR8>(v);
      Ls = xrow_sum(Ls);
      v = gate * rbf(v * __builtin_amdgcn_rcpf(Ls));
      if ((l & 8) == 0) L.ou[oslot] = v;
    }
    __syncthreads();
    const f32x4 u = *(const f32x4*)&L.ou[4 * rho];
    const f32x2 ux = *(const f32x2*)&L.ou[16 + 2 * (l >> 3)];
#pragma unroll
    for (int j = 0; j < 4; ++j)
#pragma unroll
      for (int k = 0; k < 8; ++k) y[k] = fmaf(u[j], wf[j][k], y[k]);
    float z[4];
#pragma unroll
    for (int k = 0; k < 4; ++k) z[k] = fold32(y[k], y[k + 4]);
#pragma unroll
    for (int k = 0; k < 4; ++k) z[k] = fold16(z[k], z[k]);
    PUT_QUAD(rws, yo, T, z[0], z[1], z[2], z[3], POL_Y_ST);
    if (w == 3) {
#pragma unroll
      for (int j = 0; j < 2; ++j)
#pragma unroll
        for (int k = 0; k < 8; ++k) yx[k] = fmaf(ux[j], wx[j][k], yx[k]);
#pragma unroll
      for (int k = 0; k < 4; ++k) z[k] = fold32(yx[k], yx[k + 4]);
#pragma unroll
      for (int k = 0; k < 4; ++k) { z[k] = fold16(z[k], z[k]); z[k] += dpp<ROR8>(z[k]); }
      PUT_QUAD(rws, yxo, T, z[0], z[1], z[2], z[3], POL_Y_ST);
    }
  }
  PIN();
  STAMP(9);
  if ((b >> 3) < 18) {
    const int o0 = WS_Y + t * Y_ROW + fin * 16;
    __builtin_amdgcn_s_sleep(FIN_SLEEP);
    LD4Z(pa, rws, o0, POL_Y_LD);
    WAITV1(0, pa);
    SETTLE1(rws, o0, T, true, pa, FAR_SLEEP_3, POL_Y_LD);
    STAMP(10);
    const float v = row_sum(fold16(fold32(blo(pa[0]), blo(pa[2])), fold32(bhi(pa[0]), bhi(pa[2]))));
    if ((l & 15) == 0) L.fin[w][rho] = v;
    __syncthreads();
    if (t < 4) {
      const float r = ((L.fin[0][t] + L.fin[1][t]) + (L.fin[2][t] + L.fin[3][t])) + ngate * blo(b2v);
      const unsigned pr = cvtpk(r, r); const int o = ocol * 2;
      STHZ(pr, rout, o, "");
    }
    if (b == 0 && t == 0) { const int z = WS_EPOCH; const unsigned tv = T; ST1Z(tv, rws, z, "sc1"); }
  }
}

extern "C" void kernel_launch(void* const* d_in, const int* in_sizes, int n_in, void* d_out, int out_size, void* d_ws, size_t ws_size, hipStream_t stream) {
  fused_decode<<<256, 256, 0, stream>>>((const u16*)d_in[0], (const u16*)d_in[1], (const u16*)d_in[2], (const u16*)d_in[3], (const u16*)d_in[4],
                                        (const u16*)d_in[6], (const u16*)d_in[7], (const u16*)d_in[8], (const u16*)d_in[9], (const u16*)d_in[10],
                                        (unsigned char*)d_ws, (u16*)d_out);
}
```

```cpp
#define STAMPS
#include <stdio.h>
#include <hip/hip_runtime.h>
#include <hip/hip_bf16.h>
#include <stdint.h>

typedef unsigned short u16;
typedef unsigned u32x4 __attribute__((ext_vector_type(4)));
typedef float f32x4 __attribute__((ext_vector_type(4)));
typedef float f32x2 __attribute__((ext_vector_type(2)));
typedef short s16x8 __attribute__((ext_vector_type(8)));

constexpr int DM = 576, HKV = 16, SK = 32768, DH = 128, WIN = 4096, FF = 4608, NQ = 4096;
constexpr int OOB = 0x40000000;

constexpr unsigned WS_EPOCH = 0;
constexpr unsigned WS_Q = 4096;
constexpr unsigned H_ROW = 1280, O_ROW = 1152, Y_ROW = 2304;
constexpr unsigned WS_H = WS_Q + 256 * 512;
constexpr unsigned WS_O = WS_H + 256 * H_ROW;
constexpr unsigned WS_Y = WS_O + 256 * O_ROW;
constexpr unsigned WS_END = WS_Y + 256 * Y_ROW;
constexpr unsigned SPIN_CAP = 1u << 18;
#define FAR_SLEEP_3 12
#define POL_QH_ST "sc1 nt"
#define POL_QH_LD "sc1 nt"
#define POL_O_ST "sc1"
#define POL_O_LD "sc1"
#define POL_Y_ST "sc1"
#define POL_Y_LD "sc1"
#define K0 4
#define K1 10
#define O_SLEEP 12
#define FIN_SLEEP 12

#define STAMP_PER_WG 16
#define STAMP_WORDS (256 * STAMP_PER_WG)
#define STAMP_OFF (64u << 20)
#define STAMP(k) do { __builtin_amdgcn_sched_barrier(0); asm volatile("s_memrealtime %0\n\ts_waitcnt lgkmcnt(0)" : "=s"(ts[k]) :: "memory"); __builtin_amdgcn_sched_barrier(0); } while (0)
#define PIN() __builtin_amdgcn_sched_barrier(0)

typedef u32x4 srd_t;
__device__ __forceinline__ srd_t mk(const void* p, unsigned bytes) {
  const unsigned long long a = (unsigned long long)p;
  return srd_t{(unsigned)a, (unsigned)(a >> 32), bytes, 0x00020000u};
}
#define LD4(dst, srd, voff, soff, pol) asm volatile("buffer_load_dwordx4 %0, %1, %2, %3 offen " pol : "=v"(dst) : "v"(voff), "s"(srd), "s"(soff))
#define LD4Z(dst, srd, voff, pol) asm volatile("buffer_load_dwordx4 %0, %1, %2, 0 offen " pol : "=v"(dst) : "v"(voff), "s"(srd))
#define LD1Z(dst, srd, voff, pol) asm volatile("buffer_load_dword %0, %1, %2, 0 offen " pol : "=v"(dst) : "v"(voff), "s"(srd))
#define LDHZ(dst, srd, voff, pol) asm volatile("buffer_load_ushort %0, %1, %2, 0 offen " pol : "=v"(dst) : "v"(voff), "s"(srd))
#define ST4Z(val, srd, voff, pol) asm volatile("buffer_store_dwordx4 %0, %1, %2, 0 offen " pol "\n\ts_nop 1" :: "v"(val), "v"(voff), "s"(srd) : "memory")
#define ST1Z(val, srd, voff, pol) asm volatile("buffer_store_dword %0, %1, %2, 0 offen " pol :: "v"(val), "v"(voff), "s"(srd) : "memory")
#define STHZ(val, srd, voff, pol) asm volatile("buffer_store_short %0, %1, %2, 0 offen " pol :: "v"(val), "v"(voff), "s"(srd) : "memory")
#define WAITV1(n, a) asm volatile("s_waitcnt vmcnt(" #n ")" : "+v"(a))
#define WAITV2(n, a, b) asm volatile("s_waitcnt vmcnt(" #n ")" : "+v"(a), "+v"(b))
#define WAITV3(n, a, b, c) asm volatile("s_waitcnt vmcnt(" #n ")" : "+v"(a), "+v"(b), "+v"(c))
#define WAITN(n) asm volatile("s_waitcnt vmcnt(%0)" :: "i"(n))
#define TIE(a) asm volatile("" : "+v"(a))
__device__ __forceinline__ float asf(unsigned u) { return __builtin_bit_cast(float, u); }
__device__ __forceinline__ unsigned asu(float f) { return __builtin_bit_cast(unsigned, f); }
__device__ __forceinline__ float blo(unsigned v) { return asf(v << 16); }
__device__ __forceinline__ float bhi(unsigned v) { return asf(v & 0xffff0000u); }
typedef __bf16 bf16x2_t __attribute__((ext_vector_type(2)));
__device__ __forceinline__ unsigned cvtpk(float lo, float hi) {
  return __builtin_bit_cast(unsigned, __builtin_convertvector(f32x2{lo, hi}, bf16x2_t));
}
__device__ __forceinline__ float rbf(float f) { return blo(cvtpk(f, f)); }
template <int CTRL> __device__ __forceinline__ float dpp(float x) {
  return asf(__builtin_amdgcn_mov_dpp((int)asu(x), CTRL, 0xf, 0xf, true));
}
constexpr int XOR1 = 0xB1, XOR2 = 0x4E, HMIR = 0x141, ROR8 = 0x128, ROR4 = 0x124;
__device__ __forceinline__ float xrow_sum(float x) {
  auto s = __builtin_amdgcn_permlane16_swap(asu(x), asu(x), false, false);
  x = asf(s[0]) + asf(s[1]);
  auto t = __builtin_amdgcn_permlane32_swap(asu(x), asu(x), false, false);
  return asf(t[0]) + asf(t[1]);
}
__device__ __forceinline__ float xrow_max(float x) {
  auto s = __builtin_amdgcn_permlane16_swap(asu(x), asu(x), false, false);
  x = fmaxf(asf(s[0]), asf(s[1]));
  auto t = __builtin_amdgcn_permlane32_swap(asu(x), asu(x), false, false);
  return fmaxf(asf(t[0]), asf(t[1]));
}
__device__ __forceinline__ float row_sum(float x) {
  x += dpp<XOR1>(x); x += dpp<XOR2>(x); x += dpp<HMIR>(x); x += dpp<ROR8>(x); return x;
}
__device__ __forceinline__ float row_max(float x) {
  x = fmaxf(x, dpp<XOR1>(x)); x = fmaxf(x, dpp<XOR2>(x)); x = fmaxf(x, dpp<HMIR>(x)); x = fmaxf(x, dpp<ROR8>(x)); return x;
}
__device__ __forceinline__ void fma8(float (&acc)[8], u32x4 w, float x) {
#pragma unroll
  for (int k = 0; k < 4; ++k) { acc[2 * k] = fmaf(x, blo(w[k]), acc[2 * k]); acc[2 * k + 1] = fmaf(x, bhi(w[k]), acc[2 * k + 1]); }
}
#define PUT_QUAD(r, voff, T, a, b, c, d, pol) PUT_PAIR(r, voff, T, asf(cvtpk(a, b)), asf(cvtpk(c, d)), pol)
#define PUT_PAIR(r, voff, T, a, b, pol) do { const float a_ = (a), b_ = (b); const int o_ = (voff); \
    const u32x4 v_ = {asu(a_), (T) ^ asu(b_), asu(b_), (T) ^ asu(a_)}; ST4Z(v_, r, o_, pol); } while (0)
__device__ __forceinline__ bool pair_ok(u32x4 v, unsigned T) { return ((v[1] ^ v[2]) == T) & ((v[3] ^ v[0]) == T); }
typedef unsigned long long u64;
#define BALLOT(c) __builtin_amdgcn_ballot_w64(c)
#define SETTLE1(r, o0, T, live, a, far_sleep, pol) \
  _Pragma("unroll 1") for (unsigned spins_ = 0; spins_ < SPIN_CAP; ++spins_) { \
    const u64 ma_ = BALLOT((live) & !pair_ok(a, T)); \
    if (ma_ == 0) break; \
    if (far_sleep && __popcll(ma_) > 16) __builtin_amdgcn_s_sleep(far_sleep); \
    u64 sv_; \
    asm volatile("s_mov_b64 %1, exec\n\ts_mov_b64 exec, %2\n\tbuffer_load_dwordx4 %0, %3, %4, 0 offen " pol "\n\ts_mov_b64 exec, %1\n\ts_waitcnt vmcnt(0)" \
                 : "+v"(a), "=&s"(sv_) : "s"(ma_), "v"(o0), "s"(r) : "memory"); }
#define SETTLE2(r, o0, o1, T, a, b, pol) SETTLE2F(r, o0, o1, T, a, b, 0, pol)
#define SETTLE2F(r, o0, o1, T, a, b, far_sleep, pol) \
  _Pragma("unroll 1") for (unsigned spins_ = 0; spins_ < SPIN_CAP; ++spins_) { \
    const u64 ma_ = BALLOT(!pair_ok(a, T)), mb_ = BALLOT(!pair_ok(b, T)); \
    if ((ma_ | mb_) == 0) break; \
    if (far_sleep && __popcll(ma_) + __popcll(mb_) > 32) __builtin_amdgcn_s_sleep(far_sleep); \
    u64 sv_; \
    asm volatile("s_mov_b64 %2, exec\n\ts_mov_b64 exec, %3\n\tbuffer_load_dwordx4 %0, %5, %7, 0 offen " pol "\n\t" \
                 "s_mov_b64 exec, %4\n\tbuffer_load_dwordx4 %1, %6, %7, 0 offen " pol "\n\ts_mov_b64 exec, %2\n\ts_waitcnt vmcnt(0)" \
                 : "+v"(a), "+v"(b), "=&s"(sv_) : "s"(ma_), "s"(mb_), "v"(o0), "v"(o1), "s"(r) : "memory"); }
#define SETTLE4(r, o0, o1, o2, o3, T, a, b, c, d, pol) \
  _Pragma("unroll 1") for (unsigned spins_ = 0; spins_ < SPIN_CAP; ++spins_) { \
    const u64 m0_ = BALLOT(!(pair_ok(a, T) & pair_ok(b, T))), m1_ = BALLOT(!(pair_ok(c, T) & pair_ok(d, T))); \
    if ((m0_ | m1_) == 0) break; \
    u64 sv_; \
    asm volatile("s_mov_b64 %4, exec\n\ts_mov_b64 exec, %5\n\tbuffer_load_dwordx4 %0, %7, %11, 0 offen " pol "\n\tbuffer_load_dwordx4 %1, %8, %11, 0 offen " pol "\n\t" \
                 "s_mov_b64 exec, %6\n\tbuffer_load_dwordx4 %2, %9, %11, 0 offen " pol "\n\tbuffer_load_dwordx4 %3, %10, %11, 0 offen " pol "\n\t" \
                 "s_mov_b64 exec, %4\n\ts_waitcnt vmcnt(0)" \
                 : "+v"(a), "+v"(b), "+v"(c), "+v"(d), "=&s"(sv_) : "s"(m0_), "s"(m1_), "v"(o0), "v"(o1), "v"(o2), "v"(o3), "s"(r) : "memory"); }
__device__ __forceinline__ float fold32(float a, float b) {
  auto s = __builtin_amdgcn_permlane32_swap(asu(a), asu(b), false, false);
  return asf(s[0]) + asf(s[1]);
}
__device__ __forceinline__ float fold16(float a, float b) {
  auto s = __builtin_amdgcn_permlane16_swap(asu(a), asu(b), false, false);
  return asf(s[0]) + asf(s[1]);
}
struct Lds {
  float xs[DM];
  float gp[72];
  float qred[4][64];
  float qh[2][256];
  float hb[7][292];
  float ob[4][256];
  float ml[4][2][2];
  float act[24];
  float ou[32];
  float fin[4][4];
};

__global__ void __launch_bounds__(256, 2)
fused_decode(const u16* __restrict__ x, const u16* __restrict__ kc, const u16* __restrict__ vc, const u16* __restrict__ wq,
             const u16* __restrict__ wo, const u16* __restrict__ w1, const u16* __restrict__ b1, const u16* __restrict__ w2,
             const u16* __restrict__ b2, const u16* __restrict__ gw, unsigned char* ws, u16* out, unsigned run) {
  __shared__ __attribute__((aligned(16))) Lds L;
  unsigned long long ts[STAMP_PER_WG];
  for (int i = 0; i < STAMP_PER_WG; ++i) ts[i] = 0;
  STAMP(0);
  const int b = blockIdx.x, t = threadIdx.x, l = t & 63, w = __builtin_amdgcn_readfirstlane(t >> 6);
  const int h = ((b & 7) << 1) | ((b >> 3) & 1), s = b >> 4, gid = h * 16 + s;
  const int cb = s & 3, rg = s >> 2;

  const srd_t rws = mk(ws, WS_END);
  const srd_t rx = mk(x, DM * 2), rgw = mk(gw, DM * 2);
  const srd_t rwq = mk(wq + (size_t)(144 * rg) * NQ + h * 256 + cb * 64, 143 * 8192 + 128);
  const size_t kvo = ((size_t)h * SK + (SK - WIN) + s * 256) * DH;
  const srd_t rk = mk(kc + kvo, 65536), rv = mk(vc + kvo, 65536);
  const srd_t rw1 = mk(w1 + (size_t)(36 * s) * FF + h * 288, 35 * 9216 + 576);
  const srd_t rw2 = mk(w2 + (size_t)gid * 18 * DM, 18 * 1152);
  const srd_t rwo = mk(wo + (size_t)gid * 16 * DM, 16 * 1152);
  const srd_t rb1 = mk(b1 + gid * 18, 36);

  const srd_t rb2 = mk(b2, DM * 2), rout = mk(out, DM * 2);
  unsigned epoch; u32x4 xv, gv;
  { const int z = WS_EPOCH; LD1Z(epoch, rws, z, "sc1"); }
  { const int o = t * 16; LD4Z(xv, rx, o, ""); LD4Z(gv, rgw, o, ""); }
  u32x4 aq[5];
  {
    const int vo = (8 * w + (l >> 3)) * 8192 + (l & 7) * 16;
#pragma unroll
    for (int j = 0; j < 5; ++j) LD4(aq[j], rwq, vo, j * 32 * 8192, "nt");
  }
  u32x4 a1[6];
  const int r7 = t / 36, c36 = t - r7 * 36;
  {
    const int vo = t < 252 ? r7 * 9216 + c36 * 16 : OOB;
#pragma unroll
    for (int j = 0; j < 6; ++j) LD4(a1[j], rw1, vo, j * 7 * 9216, "nt");
  }
  unsigned b1v;
  { const int o = ((t >> 4) * 4 + (t & 3)) * 2; LDHZ(b1v, rb1, o, ""); }
  u32x4 ak[16], av[16];
  const int kvoff = w * 1024 + ((l & 15) >> 2) * 256 + (l & 3) * 16 + (l >> 4) * 64;
  const int vvoff = w * 1024 + l * 16;
#pragma unroll
  for (int j = 0; j < K0; ++j) LD4(ak[j], rk, kvoff, j * 4096, "nt");
  PIN();

  WAITN(12 + K0); TIE(epoch); TIE(xv); TIE(gv);
  if (t < 72) {
    float g = 0.f, xf[8];
#pragma unroll
    for (int k = 0; k < 4; ++k) {
      xf[2 * k] = blo(xv[k]); xf[2 * k + 1] = bhi(xv[k]);
      const unsigned pr = cvtpk(xf[2 * k] * blo(gv[k]), xf[2 * k + 1] * bhi(gv[k]));
      g += blo(pr); g += bhi(pr);
    }
    *(f32x4*)&L.xs[8 * t] = f32x4{xf[0], xf[1], xf[2], xf[3]};
    *(f32x4*)&L.xs[8 * t + 4] = f32x4{xf[4], xf[5], xf[6], xf[7]};
    L.gp[t] = g;
  }
  if (t >= 250) L.act[t - 232] = 0.f;
  unsigned T = __builtin_amdgcn_readfirstlane(epoch) + 1u;
  T = T ? T : 1u;
  __syncthreads();
  STAMP(1);
  float gate, ngate;
  {
    float v = L.gp[l] + (l < 8 ? L.gp[64 + l] : 0.f);
    v = rbf(xrow_sum(row_sum(v)));
    gate = rbf(1.f / (1.f + __expf(-v)));
    ngate = rbf(1.f - gate);
  }
  float x1[6];
#pragma unroll
  for (int j = 0; j < 6; ++j) x1[j] = L.xs[min(36 * s + 7 * j + r7, DM - 1)];
  {
    float acc[8] = {0.f, 0.f, 0.f, 0.f, 0.f, 0.f, 0.f, 0.f};
    const int r0 = 144 * rg + 8 * w + (l >> 3);
    float xr[5];
#pragma unroll
    for (int j = 0; j < 5; ++j) xr[j] = L.xs[min(r0 + 32 * j, DM - 1)];
    WAITN(7 + K0); TIE(aq[0]); TIE(aq[1]); TIE(aq[2]); TIE(aq[3]); TIE(aq[4]);
#pragma unroll
    for (int j = 0; j < 5; ++j) fma8(acc, aq[j], xr[j]);
    float z[4], y[2];
#pragma unroll
    for (int k = 0; k < 4; ++k) z[k] = fold32(acc[k], acc[k + 4]);
#pragma unroll
    for (int k = 0; k < 2; ++k) { y[k] = fold16(z[k], z[k + 2]); y[k] += dpp<ROR8>(y[k]); }
    if ((l & 8) == 0) *(f32x2*)&L.qred[w][8 * (l & 7) + 2 * (l >> 4)] = f32x2{y[0], y[1]};
  }
  __syncthreads();
  {
    f32x2 a = *(const f32x2*)&L.qred[0][2 * (t & 31)];
#pragma unroll
    for (int i = 1; i < 4; ++i) a += *(const f32x2*)&L.qred[i][2 * (t & 31)];
    PUT_PAIR(rws, t < 32 ? WS_Q + gid * 512 + t * 16 : OOB, T, a[0], a[1], POL_QH_ST);
  }
#pragma unroll
  for (int j = K0; j < K1; ++j) LD4(ak[j], rk, kvoff, j * 4096, "nt");
  PIN();
  STAMP(2);
  {
    float acc[8] = {0.f, 0.f, 0.f, 0.f, 0.f, 0.f, 0.f, 0.f};
    WAITN(2 + K1); TIE(a1[0]);
#pragma unroll
    for (int j = 1; j < 6; ++j) TIE(a1[j]);
#pragma unroll
    for (int j = 0; j < 6; ++j) fma8(acc, a1[j], x1[j]);
    if (t < 252) {
      *(f32x4*)&L.hb[r7][8 * c36] = f32x4{acc[0], acc[1], acc[2], acc[3]};
      *(f32x4*)&L.hb[r7][8 * c36 + 4] = f32x4{acc[4], acc[5], acc[6], acc[7]};
    }
  }
  __syncthreads();
  {
    const int tt = min(t, 79), j = tt / 5, c0 = 18 * j + 4 * (tt - 5 * j);
    f32x2 a = *(const f32x2*)&L.hb[0][c0], c = *(const f32x2*)&L.hb[0][c0 + 2];
#pragma unroll
    for (int i = 1; i < 7; ++i) { a += *(const f32x2*)&L.hb[i][c0]; c += *(const f32x2*)&L.hb[i][c0 + 2]; }
    PUT_QUAD(rws, t < 80 ? WS_H + gid * H_ROW + t * 16 : OOB, T, a[0], a[1], c[0], c[1], POL_QH_ST);
  }
#pragma unroll
  for (int j = K1; j < 16; ++j) LD4(ak[j], rk, kvoff, j * 4096, "nt");
  const int qo0 = WS_Q + (h * 16 + (t >> 7) * 8 + ((t & 127) >> 5)) * 512 + (t & 31) * 16, qo1 = qo0 + 4 * 512;
  u32x4 pa, pb, ph;
  LD4Z(pa, rws, qo0, POL_QH_LD); LD4Z(pb, rws, qo1, POL_QH_LD);
#pragma unroll
  for (int j = 0; j < 12; ++j) LD4(av[j], rv, vvoff, j * 4096, "nt");
  const int ho = t < 80 ? WS_H + (h * 16 + (t & 15)) * H_ROW + (s * 5 + (t >> 4)) * 16 : OOB;
  PIN();
  STAMP(3);
  WAITV2(12, pa, pb); TIE(b1v);
#pragma unroll
  for (int j = 0; j < 16; ++j) TIE(ak[j]);
  SETTLE2(rws, qo0, qo1, T, pa, pb, POL_QH_LD);
  *(f32x2*)&L.qh[t >> 7][2 * (t & 127)] = f32x2{asf(pa[0]) + asf(pb[0]), asf(pa[2]) + asf(pb[2])};
  __syncthreads();
  STAMP(4);
  u32x4 qf;
  {
    const int o = ((l >> 3) & 1) * 128 + ((l & 3) + 4 * (l >> 4)) * 8;
    const f32x4 a0 = *(const f32x4*)&L.qh[0][o] + *(const f32x4*)&L.qh[1][o];
    const f32x4 a1_ = *(const f32x4*)&L.qh[0][o + 4] + *(const f32x4*)&L.qh[1][o + 4];
    qf = u32x4{cvtpk(a0[0], a0[1]), cvtpk(a0[2], a0[3]), cvtpk(a1_[0], a1_[1]), cvtpk(a1_[2], a1_[3])};
  }
  constexpr float C2 = 0.08838834764831845f * 1.4426950408889634f;
  u32x4 a2[5], ao[4], a2x[3], aox[2];
  const int rho = l >> 4;
  const int wvo = rho * 1152 + w * 256 + (l & 15) * 16;
  const int x2o = w == 3 ? (l >> 3) * 1152 + 1024 + (l & 7) * 16 : OOB;
  float sc[16];
  {
    f32x4 d[16];
#pragma unroll
    for (int j = 0; j < 16; ++j)
      d[j] = __builtin_amdgcn_mfma_f32_16x16x32_bf16(__builtin_bit_cast(s16x8, ak[j]), __builtin_bit_cast(s16x8, qf), f32x4{0.f, 0.f, 0.f, 0.f}, 0, 0, 0);
    PIN();
#pragma unroll
    for (int j = 0; j < 16; ++j) sc[j] = (dpp<0x00>(d[j][0]) + dpp<0x55>(d[j][1])) + (dpp<0xAA>(d[j][2]) + dpp<0xFF>(d[j][3]));
  }
  float mx = -INFINITY;
#pragma unroll
  for (int j = 0; j < 16; ++j) mx = fmaxf(mx, sc[j]);
  mx = xrow_max(mx) * C2;
  float lsum = 0.f;
  unsigned pk[16];
#pragma unroll
  for (int j = 0; j < 16; ++j) { const float p = __builtin_amdgcn_exp2f(fmaf(sc[j], C2, -mx)); lsum += p; pk[j] = cvtpk(p, p); }
  lsum = xrow_sum(lsum);
  u32x4 msk;
  {
    const unsigned half = (l & 1) ? 0xffff0000u : 0x0000ffffu;
#pragma unroll
    for (int k = 0; k < 4; ++k) msk[k] = (((l & 7) >> 1) == k) ? half : 0u;
  }
  PIN();
  STAMP(5);
  f32x4 oacc = {0.f, 0.f, 0.f, 0.f};
#define PV_STEP(j, n) { const u32x4 pf = {pk[j] & msk[0], pk[j] & msk[1], pk[j] & msk[2], pk[j] & msk[3]}; WAITV1(n, av[j]); \
    oacc = __builtin_amdgcn_mfma_f32_16x16x32_bf16(__builtin_bit_cast(s16x8, pf), __builtin_bit_cast(s16x8, av[j]), oacc, 0, 0, 0); PIN(); }
  LD4(av[12], rv, vvoff, 12 * 4096, "nt"); LD4(av[13], rv, vvoff, 13 * 4096, "nt");
  LD4(av[14], rv, vvoff, 14 * 4096, "nt"); LD4(av[15], rv, vvoff, 15 * 4096, "nt");
  PV_STEP(0, 15)  LD4Z(ph, rws, ho, POL_QH_LD);
  PV_STEP(1, 15)  LD4(a2[0], rw2, wvo, 0, "nt");
  PV_STEP(2, 15)  LD4(a2[1], rw2, wvo, 4608, "nt");
  PV_STEP(3, 15)  LD4(a2[2], rw2, wvo, 2 * 4608, "nt");
  PV_STEP(4, 15)  LD4(a2[3], rw2, wvo, 3 * 4608, "nt");
  PV_STEP(5, 15)  LD4(a2[4], rw2, wvo, 4 * 4608, "nt");
  PV_STEP(6, 15)  LD4(a2x[0], rw2, x2o, 0, "nt");
  PV_STEP(7, 15)  LD4(a2x[1], rw2, x2o, 9216, "nt");
  PV_STEP(8, 15)  LD4(a2x[2], rw2, x2o, 2 * 9216, "nt");
  PV_STEP(9, 15)  LD4(ao[0], rwo, wvo, 0, "nt");
  PV_STEP(10, 15) LD4(ao[1], rwo, wvo, 4608, "nt");
  PV_STEP(11, 15) LD4(ao[2], rwo, wvo, 2 * 4608, "nt");
  PV_STEP(12, 15) LD4(ao[3], rwo, wvo, 3 * 4608, "nt");
  PV_STEP(13, 15) LD4(aox[0], rwo, x2o, 0, "nt");
  PV_STEP(14, 15) LD4(aox[1], rwo, x2o, 9216, "nt");
  PV_STEP(15, 15)
  *(f32x4*)&L.ob[w][(l >> 5) * 128 + (l & 15) * 8 + ((l >> 4) & 1) * 4] = oacc;
  if ((l & 55) == 0) *(f32x2*)&L.ml[w][l >> 3][0] = f32x2{mx, lsum};
  STAMP(6);
  __syncthreads();
  {
    const int hh = t < 64 ? t >> 5 : t & 1, oi = 4 * l;
    const f32x2 q0 = *(const f32x2*)&L.ml[0][hh][0], q1 = *(const f32x2*)&L.ml[1][hh][0], q2 = *(const f32x2*)&L.ml[2][hh][0], q3 = *(const f32x2*)&L.ml[3][hh][0];
    const f32x4 o0 = *(const f32x4*)&L.ob[0][oi], o1 = *(const f32x4*)&L.ob[1][oi], o2 = *(const f32x4*)&L.ob[2][oi], o3 = *(const f32x4*)&L.ob[3][oi];
    const float M = fmaxf(fmaxf(q0[0], q1[0]), fmaxf(q2[0], q3[0]));
    const float e0 = __builtin_amdgcn_exp2f(q0[0] - M), e1 = __builtin_amdgcn_exp2f(q1[0] - M), e2 = __builtin_amdgcn_exp2f(q2[0] - M), e3 = __builtin_amdgcn_exp2f(q3[0] - M);
    const f32x4 v = e0 * o0 + e1 * o1 + e2 * o2 + e3 * o3;
    const float ls = e0 * q0[1] + e1 * q1[1] + e2 * q2[1] + e3 * q3[1];
    const bool iso = t < 64;
    PUT_PAIR(rws, t < 66 ? WS_O + gid * O_ROW + t * 16 : OOB, T, iso ? asf(cvtpk(v[0], v[1])) : M, iso ? asf(cvtpk(v[2], v[3])) : ls, POL_O_ST);
  }
  PIN();
  STAMP(7);
  const int fin = (b & 7) * 18 + (b >> 3);
  const int ocol = (fin < 128 ? (fin >> 5) * 128 + (fin & 15) * 8 + ((fin >> 4) & 1) * 4 : 512 + (fin & 7) * 8 + ((fin >> 3) & 1) * 4) + (t & 3);
  unsigned b2v;
  { const int o = (b >> 3) < 18 ? ocol * 2 : OOB; LDHZ(b2v, rb2, o, ""); }
  WAITV1(16, ph);
  SETTLE1(rws, ho, T, t < 80, ph, 0, POL_QH_LD);
  {
    const float s0 = row_sum(blo(ph[0])), s1 = row_sum(bhi(ph[0])), s2 = row_sum(blo(ph[2])), s3 = row_sum(bhi(ph[2]));
    const float sv = (t & 2) ? ((t & 1) ? s3 : s2) : ((t & 1) ? s1 : s0);
    const float hv = rbf(rbf(sv) + blo(b1v));
    const float z = 0.7978845608028654f * (hv + 0.044715f * hv * hv * hv);
    const float th = 1.f - 2.f / (1.f + __expf(2.f * z));
    const int f = 4 * (t >> 4) + (t & 15);
    if ((t & 15) < 4 && f < 18) L.act[f] = ngate * rbf(0.5f * hv * (1.f + th));
  }
  __syncthreads();
  STAMP(12);
  float y[8] = {0.f, 0.f, 0.f, 0.f, 0.f, 0.f, 0.f, 0.f}, yx[8] = {0.f, 0.f, 0.f, 0.f, 0.f, 0.f, 0.f, 0.f};
  {
    float u[5], ux[3];
#pragma unroll
    for (int j = 0; j < 5; ++j) u[j] = L.act[4 * j + rho];
#pragma unroll
    for (int j = 0; j < 3; ++j) ux[j] = L.act[8 * j + (l >> 3)];
    WAITV1(8, a2[0]);
#pragma unroll
    for (int j = 1; j < 5; ++j) TIE(a2[j]);
#pragma unroll
    for (int j = 0; j < 3; ++j) TIE(a2x[j]);
#pragma unroll
    for (int j = 0; j < 5; ++j) fma8(y, a2[j], u[j]);
    if (w == 3) {
#pragma unroll
      for (int j = 0; j < 3; ++j) fma8(yx, a2x[j], ux[j]);
    }
#pragma unroll
    for (int k = 0; k < 8; ++k) { TIE(y[k]); TIE(yx[k]); }
  }
  PIN();
  STAMP(13);
  {
    const int ob_ = w == 0 ? WS_O + (h * 16 + (l >> 2)) * O_ROW : OOB;
    const int oo = ob_ + s * 64 + (l & 3) * 16, om = ob_ + 1024 + (s >> 3) * 16;
    const int orow = 4 * (l & 3) + rho;
    int oslot = (l & 4) ? 16 + (orow & 7) * 2 + (orow >> 3) : rho * 4 + (l & 3);
    int yo = (l & 16) == 0 ? WS_Y + b * Y_ROW + (32 * w + 16 * (l >> 5) + (l & 15)) * 16 : OOB;
    int yxo = (l & 24) == 0 ? WS_Y + b * Y_ROW + (128 + 8 * (l >> 5) + (l & 7)) * 16 : OOB;
    TIE(oslot); TIE(yo); TIE(yxo);
    u32x4 p0, m0;
    LD4Z(p0, rws, oo, POL_O_LD); LD4Z(m0, rws, om, POL_O_LD);
    WAITV1(2, ao[0]); TIE(ao[1]); TIE(ao[2]); TIE(ao[3]); TIE(aox[0]); TIE(aox[1]); TIE(b2v);
    float wf[4][8], wx[2][8];
#pragma unroll
    for (int j = 0; j < 4; ++j)
#pragma unroll
      for (int k = 0; k < 4; ++k) { wf[j][2 * k] = blo(ao[j][k]); wf[j][2 * k + 1] = bhi(ao[j][k]); TIE(wf[j][2 * k]); TIE(wf[j][2 * k + 1]); }
#pragma unroll
    for (int j = 0; j < 2; ++j)
#pragma unroll
      for (int k = 0; k < 4; ++k) { wx[j][2 * k] = blo(aox[j][k]); wx[j][2 * k + 1] = bhi(aox[j][k]); TIE(wx[j][2 * k]); TIE(wx[j][2 * k + 1]); }
    PIN();
    STAMP(14);
    WAITV2(0, p0, m0);
    STAMP(15);
    if (w == 0) {
      SETTLE2(rws, oo, om, T, p0, m0, POL_O_LD);
      STAMP(8);
      float M = asf(m0[0]);
      M = fmaxf(M, dpp<ROR4>(M)); M = fmaxf(M, dpp<ROR8>(M)); M = xrow_max(M);
      const float e = __builtin_amdgcn_exp2f(asf(m0[0]) - M);
      float Ls = e * asf(m0[2]);
      float v = fold16(fold32(e * blo(p0[0]), e * blo(p0[2])), fold32(e * bhi(p0[0]), e * bhi(p0[2])));
      Ls += dpp<ROR4>(Ls); v += dpp<ROR4>(v);
      Ls += dpp<ROR8>(Ls); v += dpp<ROR8>(v);
      Ls = xrow_sum(Ls);
      v = gate * rbf(v * __builtin_amdgcn_rcpf(Ls));
      if ((l & 8) == 0) L.ou[oslot] = v;
    }
    __syncthreads();
    const f32x4 u = *(const f32x4*)&L.ou[4 * rho];
    const f32x2 ux = *(const f32x2*)&L.ou[16 + 2 * (l >> 3)];
#pragma unroll
    for (int j = 0; j < 4; ++j)
#pragma unroll
      for (int k = 0; k < 8; ++k) y[k] = fmaf(u[j], wf[j][k], y[k]);
    float z[4];
#pragma unroll
    for (int k = 0; k < 4; ++k) z[k] = fold32(y[k], y[k + 4]);
#pragma unroll
    for (int k = 0; k < 4; ++k) z[k] = fold16(z[k], z[k]);
    PUT_QUAD(rws, yo, T, z[0], z[1], z[2], z[3], POL_Y_ST);
    if (w == 3) {
#pragma unroll
      for (int j = 0; j < 2; ++j)
#pragma unroll
        for (int k = 0; k < 8; ++k) yx[k] = fmaf(ux[j], wx[j][k], yx[k]);
#pragma unroll
      for (int k = 0; k < 4; ++k) z[k] = fold32(yx[k], yx[k + 4]);
#pragma unroll
      for (int k = 0; k < 4; ++k) { z[k] = fold16(z[k], z[k]); z[k] += dpp<ROR8>(z[k]); }
      PUT_QUAD(rws, yxo, T, z[0], z[1], z[2], z[3], POL_Y_ST);
    }
  }
  PIN();
  STAMP(9);
  if ((b >> 3) < 18) {
    const int o0 = WS_Y + t * Y_ROW + fin * 16;
    __builtin_amdgcn_s_sleep(FIN_SLEEP);
    LD4Z(pa, rws, o0, POL_Y_LD);
    WAITV1(0, pa);
    SETTLE1(rws, o0, T, true, pa, FAR_SLEEP_3, POL_Y_LD);
    STAMP(10);
    const float v = row_sum(fold16(fold32(blo(pa[0]), blo(pa[2])), fold32(bhi(pa[0]), bhi(pa[2]))));
    if ((l & 15) == 0) L.fin[w][rho] = v;
    __syncthreads();
    if (t < 4) {
      const float r = ((L.fin[0][t] + L.fin[1][t]) + (L.fin[2][t] + L.fin[3][t])) + ngate * blo(b2v);
      const unsigned pr = cvtpk(r, r); const int o = ocol * 2;
      STHZ(pr, rout, o, "");
    }
    if (b == 0 && t == 0) { const int z = WS_EPOCH; const unsigned tv = T; ST1Z(tv, rws, z, "sc1"); }
  }
  STAMP(11);
  if (t == 0) { unsigned long long* sp = (unsigned long long*)(ws + STAMP_OFF + run * 65536) + b * STAMP_PER_WG; for (int i = 0; i < STAMP_PER_WG; ++i) sp[i] = ts[i] ? ts[i] : ts[9]; }
}

__global__ void show(const unsigned char* ws, int nrun) {
  for (int r = 0; r < nrun; ++r) {
    const unsigned long long* sp = (const unsigned long long*)(ws + STAMP_OFF + r * 65536);
    unsigned long long t0 = ~0ull;
    for (int w = 0; w < 256; ++w) t0 = sp[w * STAMP_PER_WG] < t0 ? sp[w * STAMP_PER_WG] : t0;
    for (int k = 0; k < STAMP_PER_WG; ++k) {
      int mx[8], mn = 1 << 30; long sum = 0;
      int m0 = 0, m1 = 0, m2 = 0, m3 = 0, m4 = 0, m5 = 0, m6 = 0, m7 = 0;
      for (int w = 0; w < 256; ++w) { int v = (int)(sp[w * STAMP_PER_WG + k] - t0); mn = v < mn ? v : mn; sum += v;
        switch (w & 7) { case 0: m0 = v > m0 ? v : m0; break; case 1: m1 = v > m1 ? v : m1; break; case 2: m2 = v > m2 ? v : m2; break; case 3: m3 = v > m3 ? v : m3; break;
                         case 4: m4 = v > m4 ? v : m4; break; case 5: m5 = v > m5 ? v : m5; break; case 6: m6 = v > m6 ? v : m6; break; default: m7 = v > m7 ? v : m7; } }
      printf("run %d s%d min %d avg %d xcdmax %d %d %d %d %d %d %d %d\n", r, k, mn, (int)(sum / 256), m0, m1, m2, m3, m4, m5, m6, m7);
    }
  }
}
extern "C" void kernel_launch(void* const* d_in, const int* in_sizes, int n_in, void* d_out, int out_size, void* d_ws, size_t ws_size, hipStream_t stream) {
  for (int i = 0; i < n_in; ++i) fprintf(stderr, "in%d %p\n", i, d_in[i]);
  fprintf(stderr, "out %p ws %p\n", d_out, d_ws);
  hipPointerAttribute_t at; if (hipPointerGetAttributes(&at, d_ws) == hipSuccess) fprintf(stderr, "ws type %d managed %d flags %u\n", (int)at.type, at.isManaged, at.allocationFlags);
  if (hipPointerGetAttributes(&at, d_in[1]) == hipSuccess) fprintf(stderr, "k type %d managed %d flags %u\n", (int)at.type, at.isManaged, at.allocationFlags);
  for (unsigned r = 0; r < 4; ++r)
  fused_decode<<<256, 256, 0, stream>>>((const u16*)d_in[0], (const u16*)d_in[1], (const u16*)d_in[2], (const u16*)d_in[3], (const u16*)d_in[4],
                                        (const u16*)d_in[6], (const u16*)d_in[7], (const u16*)d_in[8], (const u16*)d_in[9], (const u16*)d_in[10],
                                        (unsigned char*)d_ws, (u16*)d_out, r);
  show<<<1, 1, 0, stream>>>((const unsigned char*)d_ws, 4);
}
```

```cpp
#define STAMPS
#include <hip/hip_runtime.h>
#include <hip/hip_bf16.h>
#include <stdint.h>

typedef unsigned short u16;
typedef unsigned u32x4 __attribute__((ext_vector_type(4)));
typedef float f32x4 __attribute__((ext_vector_type(4)));
typedef float f32x2 __attribute__((ext_vector_type(2)));
typedef short s16x8 __attribute__((ext_vector_type(8)));

constexpr int DM = 576, HKV = 16, SK = 32768, DH = 128, WIN = 4096, FF = 4608, NQ = 4096;
constexpr int OOB = 0x40000000;

constexpr unsigned WS_EPOCH = 0;
constexpr unsigned WS_Q = 4096;
constexpr unsigned H_ROW = 1280, O_ROW = 1152, Y_ROW = 2304;
constexpr unsigned WS_H = WS_Q + 256 * 512;
constexpr unsigned WS_O = WS_H + 256 * H_ROW;
constexpr unsigned WS_Y = WS_O + 256 * O_ROW;
constexpr unsigned WS_END = WS_Y + 256 * Y_ROW;
constexpr unsigned SPIN_CAP = 1u << 18;
#define FAR_SLEEP_3 12
#define POL_QH_ST "sc1 nt"
#define POL_QH_LD "sc1 nt"
#define POL_O_ST "sc1"
#define POL_O_LD "sc1"
#define POL_Y_ST "sc1"
#define POL_Y_LD "sc1"
#define K0 4
#define K1 10
#define O_SLEEP 12
#define FIN_SLEEP 12

#define STAMP_PER_WG 16
#define STAMP_WORDS (256 * STAMP_PER_WG)
#define STAMP_OFF (64u << 20)
#define STAMP(k) do { __builtin_amdgcn_sched_barrier(0); asm volatile("s_memrealtime %0\n\ts_waitcnt lgkmcnt(0)" : "=s"(ts[k]) :: "memory"); __builtin_amdgcn_sched_barrier(0); } while (0)
#define PIN() __builtin_amdgcn_sched_barrier(0)

typedef u32x4 srd_t;
__device__ __forceinline__ srd_t mk(const void* p, unsigned bytes) {
  const unsigned long long a = (unsigned long long)p;
  return srd_t{(unsigned)a, (unsigned)(a >> 32), bytes, 0x00020000u};
}
#define LD4(dst, srd, voff, soff, pol) asm volatile("buffer_load_dwordx4 %0, %1, %2, %3 offen " pol : "=v"(dst) : "v"(voff), "s"(srd), "s"(soff))
#define LD4Z(dst, srd, voff, pol) asm volatile("buffer_load_dwordx4 %0, %1, %2, 0 offen " pol : "=v"(dst) : "v"(voff), "s"(srd))
#define LD1Z(dst, srd, voff, pol) asm volatile("buffer_load_dword %0, %1, %2, 0 offen " pol : "=v"(dst) : "v"(voff), "s"(srd))
#define LDHZ(dst, srd, voff, pol) asm volatile("buffer_load_ushort %0, %1, %2, 0 offen " pol : "=v"(dst) : "v"(voff), "s"(srd))
#define ST4Z(val, srd, voff, pol) asm volatile("buffer_store_dwordx4 %0, %1, %2, 0 offen " pol "\n\ts_nop 1" :: "v"(val), "v"(voff), "s"(srd) : "memory")
#define ST1Z(val, srd, voff, pol) asm volatile("buffer_store_dword %0, %1, %2, 0 offen " pol :: "v"(val), "v"(voff), "s"(srd) : "memory")
#define STHZ(val, srd, voff, pol) asm volatile("buffer_store_short %0, %1, %2, 0 offen " pol :: "v"(val), "v"(voff), "s"(srd) : "memory")
#define WAITV1(n, a) asm volatile("s_waitcnt vmcnt(" #n ")" : "+v"(a))
#define WAITV2(n, a, b) asm volatile("s_waitcnt vmcnt(" #n ")" : "+v"(a), "+v"(b))
#define WAITV3(n, a, b, c) asm volatile("s_waitcnt vmcnt(" #n ")" : "+v"(a), "+v"(b), "+v"(c))
#define WAITN(n) asm volatile("s_waitcnt vmcnt(%0)" :: "i"(n))
#define TIE(a) asm volatile("" : "+v"(a))
__device__ __forceinline__ float asf(unsigned u) { return __builtin_bit_cast(float, u); }
__device__ __forceinline__ unsigned asu(float f) { return __builtin_bit_cast(unsigned, f); }
__device__ __forceinline__ float blo(unsigned v) { return asf(v << 16); }
__device__ __forceinline__ float bhi(unsigned v) { return asf(v & 0xffff0000u); }
typedef __bf16 bf16x2_t __attribute__((ext_vector_type(2)));
__device__ __forceinline__ unsigned cvtpk(float lo, float hi) {
  return __builtin_bit_cast(unsigned, __builtin_convertvector(f32x2{lo, hi}, bf16x2_t));
}
__device__ __forceinline__ float rbf(float f) { return blo(cvtpk(f, f)); }
template <int CTRL> __device__ __forceinline__ float dpp(float x) {
  return asf(__builtin_amdgcn_mov_dpp((int)asu(x), CTRL, 0xf, 0xf, true));
}
constexpr int XOR1 = 0xB1, XOR2 = 0x4E, HMIR = 0x141, ROR8 = 0x128, ROR4 = 0x124;
__device__ __forceinline__ float xrow_sum(float x) {
  auto s = __builtin_amdgcn_permlane16_swap(asu(x), asu(x), false, false);
  x = asf(s[0]) + asf(s[1]);
  auto t = __builtin_amdgcn_permlane32_swap(asu(x), asu(x), false, false);
  return asf(t[0]) + asf(t[1]);
}
__device__ __forceinline__ float xrow_max(float x) {
  auto s = __builtin_amdgcn_permlane16_swap(asu(x), asu(x), false, false);
  x = fmaxf(asf(s[0]), asf(s[1]));
  auto t = __builtin_amdgcn_permlane32_swap(asu(x), asu(x), false, false);
  return fmaxf(asf(t[0]), asf(t[1]));
}
__device__ __forceinline__ float row_sum(float x) {
  x += dpp<XOR1>(x); x += dpp<XOR2>(x); x += dpp<HMIR>(x); x += dpp<ROR8>(x); return x;
}
__device__ __forceinline__ float row_max(float x) {
  x = fmaxf(x, dpp<XOR1>(x)); x = fmaxf(x, dpp<XOR2>(x)); x = fmaxf(x, dpp<HMIR>(x)); x = fmaxf(x, dpp<ROR8>(x)); return x;
}
__device__ __forceinline__ void fma8(float (&acc)[8], u32x4 w, float x) {
#pragma unroll
  for (int k = 0; k < 4; ++k) { acc[2 * k] = fmaf(x, blo(w[k]), acc[2 * k]); acc[2 * k + 1] = fmaf(x, bhi(w[k]), acc[2 * k + 1]); }
}
#define PUT_QUAD(r, voff, T, a, b, c, d, pol) PUT_PAIR(r, voff, T, asf(cvtpk(a, b)), asf(cvtpk(c, d)), pol)
#define PUT_PAIR(r, voff, T, a, b, pol) do { const float a_ = (a), b_ = (b); const int o_ = (voff); \
    const u32x4 v_ = {asu(a_), (T) ^ asu(b_), asu(b_), (T) ^ asu(a_)}; ST4Z(v_, r, o_, pol); } while (0)
__device__ __forceinline__ bool pair_ok(u32x4 v, unsigned T) { return ((v[1] ^ v[2]) == T) & ((v[3] ^ v[0]) == T); }
typedef unsigned long long u64;
#define BALLOT(c) __builtin_amdgcn_ballot_w64(c)
#define SETTLE1(r, o0, T, live, a, far_sleep, pol) \
  _Pragma("unroll 1") for (unsigned spins_ = 0; spins_ < SPIN_CAP; ++spins_) { \
    const u64 ma_ = BALLOT((live) & !pair_ok(a, T)); \
    if (ma_ == 0) break; \
    if (far_sleep && __popcll(ma_) > 16) __builtin_amdgcn_s_sleep(far_sleep); \
    u64 sv_; \
    asm volatile("s_mov_b64 %1, exec\n\ts_mov_b64 exec, %2\n\tbuffer_load_dwordx4 %0, %3, %4, 0 offen " pol "\n\ts_mov_b64 exec, %1\n\ts_waitcnt vmcnt(0)" \
                 : "+v"(a), "=&s"(sv_) : "s"(ma_), "v"(o0), "s"(r) : "memory"); }
#define SETTLE2(r, o0, o1, T, a, b, pol) SETTLE2F(r, o0, o1, T, a, b, 0, pol)
#define SETTLE2F(r, o0, o1, T, a, b, far_sleep, pol) \
  _Pragma("unroll 1") for (unsigned spins_ = 0; spins_ < SPIN_CAP; ++spins_) { \
    const u64 ma_ = BALLOT(!pair_ok(a, T)), mb_ = BALLOT(!pair_ok(b, T)); \
    if ((ma_ | mb_) == 0) break; \
    if (far_sleep && __popcll(ma_) + __popcll(mb_) > 32) __builtin_amdgcn_s_sleep(far_sleep); \
    u64 sv_; \
    asm volatile("s_mov_b64 %2, exec\n\ts_mov_b64 exec, %3\n\tbuffer_load_dwordx4 %0, %5, %7, 0 offen " pol "\n\t" \
                 "s_mov_b64 exec, %4\n\tbuffer_load_dwordx4 %1, %6, %7, 0 offen " pol "\n\ts_mov_b64 exec, %2\n\ts_waitcnt vmcnt(0)" \
                 : "+v"(a), "+v"(b), "=&s"(sv_) : "s"(ma_), "s"(mb_), "v"(o0), "v"(o1), "s"(r) : "memory"); }
#define SETTLE4(r, o0, o1, o2, o3, T, a, b, c, d, pol) \
  _Pragma("unroll 1") for (unsigned spins_ = 0; spins_ < SPIN_CAP; ++spins_) { \
    const u64 m0_ = BALLOT(!(pair_ok(a, T) & pair_ok(b, T))), m1_ = BALLOT(!(pair_ok(c, T) & pair_ok(d, T))); \
    if ((m0_ | m1_) == 0) break; \
    u64 sv_; \
    asm volatile("s_mov_b64 %4, exec\n\ts_mov_b64 exec, %5\n\tbuffer_load_dwordx4 %0, %7, %11, 0 offen " pol "\n\tbuffer_load_dwordx4 %1, %8, %11, 0 offen " pol "\n\t" \
                 "s_mov_b64 exec, %6\n\tbuffer_load_dwordx4 %2, %9, %11, 0 offen " pol "\n\tbuffer_load_dwordx4 %3, %10, %11, 0 offen " pol "\n\t" \
                 "s_mov_b64 exec, %4\n\ts_waitcnt vmcnt(0)" \
                 : "+v"(a), "+v"(b), "+v"(c), "+v"(d), "=&s"(sv_) : "s"(m0_), "s"(m1_), "v"(o0), "v"(o1), "v"(o2), "v"(o3), "s"(r) : "memory"); }
__device__ __forceinline__ float fold32(float a, float b) {
  auto s = __builtin_amdgcn_permlane32_swap(asu(a), asu(b), false, false);
  return asf(s[0]) + asf(s[1]);
}
__device__ __forceinline__ float fold16(float a, float b) {
  auto s = __builtin_amdgcn_permlane16_swap(asu(a), asu(b), false, false);
  return asf(s[0]) + asf(s[1]);
}
struct Lds {
  float xs[DM];
  float gp[72];
  float qred[4][64];
  float qh[2][256];
  float hb[7][292];
  float ob[4][256];
  float ml[4][2][2];
  float act[24];
  float ou[32];
  float fin[4][4];
};

__global__ void __launch_bounds__(256, 2)
fused_decode(const u16* __restrict__ x, const u16* __restrict__ kc, const u16* __restrict__ vc, const u16* __restrict__ wq,
             const u16* __restrict__ wo, const u16* __restrict__ w1, const u16* __restrict__ b1, const u16* __restrict__ w2,
             const u16* __restrict__ b2, const u16* __restrict__ gw, unsigned char* ws, u16* out) {
  __shared__ __attribute__((aligned(16))) Lds L;
  unsigned long long ts[STAMP_PER_WG];
  for (int i = 0; i < STAMP_PER_WG; ++i) ts[i] = 0;
  STAMP(0);
  const int b = blockIdx.x, t = threadIdx.x, l = t & 63, w = __builtin_amdgcn_readfirstlane(t >> 6);
  const int h = ((b & 7) << 1) | ((b >> 3) & 1), s = b >> 4, gid = h * 16 + s;
  const int cb = s & 3, rg = s >> 2;

  const srd_t rws = mk(ws, WS_END);
  const srd_t rx = mk(x, DM * 2), rgw = mk(gw, DM * 2);
  const srd_t rwq = mk(wq + (size_t)(144 * rg) * NQ + h * 256 + cb * 64, 143 * 8192 + 128);
  const size_t kvo = ((size_t)h * SK + (SK - WIN) + s * 256) * DH;
  const srd_t rk = mk(kc + kvo, 65536), rv = mk(vc + kvo, 65536);
  const srd_t rw1 = mk(w1 + (size_t)(36 * s) * FF + h * 288, 35 * 9216 + 576);
  const srd_t rw2 = mk(w2 + (size_t)gid * 18 * DM, 18 * 1152);
  const srd_t rwo = mk(wo + (size_t)gid * 16 * DM, 16 * 1152);
  const srd_t rb1 = mk(b1 + gid * 18, 36);

  const srd_t rb2 = mk(b2, DM * 2), rout = mk(out, DM * 2);
  unsigned epoch; u32x4 xv, gv;
  { const int z = WS_EPOCH; LD1Z(epoch, rws, z, "sc1"); }
  { const int o = t * 16; LD4Z(xv, rx, o, ""); LD4Z(gv, rgw, o, ""); }
  u32x4 aq[5];
  {
    const int vo = (8 * w + (l >> 3)) * 8192 + (l & 7) * 16;
#pragma unroll
    for (int j = 0; j < 5; ++j) LD4(aq[j], rwq, vo, j * 32 * 8192, "nt");
  }
  u32x4 a1[6];
  const int r7 = t / 36, c36 = t - r7 * 36;
  {
    const int vo = t < 252 ? r7 * 9216 + c36 * 16 : OOB;
#pragma unroll
    for (int j = 0; j < 6; ++j) LD4(a1[j], rw1, vo, j * 7 * 9216, "nt");
  }
  unsigned b1v;
  { const int o = ((t >> 4) * 4 + (t & 3)) * 2; LDHZ(b1v, rb1, o, ""); }
  u32x4 ak[16], av[16];
  const int kvoff = w * 1024 + ((l & 15) >> 2) * 256 + (l & 3) * 16 + (l >> 4) * 64;
  const int vvoff = w * 1024 + l * 16;
#pragma unroll
  for (int j = 0; j < K0; ++j) LD4(ak[j], rk, kvoff, j * 4096, "nt");
  PIN();

  WAITN(12 + K0); TIE(epoch); TIE(xv); TIE(gv);
  if (t < 72) {
    float g = 0.f, xf[8];
#pragma unroll
    for (int k = 0; k < 4; ++k) {
      xf[2 * k] = blo(xv[k]); xf[2 * k + 1] = bhi(xv[k]);
      const unsigned pr = cvtpk(xf[2 * k] * blo(gv[k]), xf[2 * k + 1] * bhi(gv[k]));
      g += blo(pr); g += bhi(pr);
    }
    *(f32x4*)&L.xs[8 * t] = f32x4{xf[0], xf[1], xf[2], xf[3]};
    *(f32x4*)&L.xs[8 * t + 4] = f32x4{xf[4], xf[5], xf[6], xf[7]};
    L.gp[t] = g;
  }
  if (t >= 250) L.act[t - 232] = 0.f;
  unsigned T = __builtin_amdgcn_readfirstlane(epoch) + 1u;
  T = T ? T : 1u;
  __syncthreads();
  STAMP(1);
  float gate, ngate;
  {
    float v = L.gp[l] + (l < 8 ? L.gp[64 + l] : 0.f);
    v = rbf(xrow_sum(row_sum(v)));
    gate = rbf(1.f / (1.f + __expf(-v)));
    ngate = rbf(1.f - gate);
  }
  float x1[6];
#pragma unroll
  for (int j = 0; j < 6; ++j) x1[j] = L.xs[min(36 * s + 7 * j + r7, DM - 1)];
  {
    float acc[8] = {0.f, 0.f, 0.f, 0.f, 0.f, 0.f, 0.f, 0.f};
    const int r0 = 144 * rg + 8 * w + (l >> 3);
    float xr[5];
#pragma unroll
    for (int j = 0; j < 5; ++j) xr[j] = L.xs[min(r0 + 32 * j, DM - 1)];
    WAITN(7 + K0); TIE(aq[0]); TIE(aq[1]); TIE(aq[2]); TIE(aq[3]); TIE(aq[4]);
#pragma unroll
    for (int j = 0; j < 5; ++j) fma8(acc, aq[j], xr[j]);
    float z[4], y[2];
#pragma unroll
    for (int k = 0; k < 4; ++k) z[k] = fold32(acc[k], acc[k + 4]);
#pragma unroll
    for (int k = 0; k < 2; ++k) { y[k] = fold16(z[k], z[k + 2]); y[k] += dpp<ROR8>(y[k]); }
    if ((l & 8) == 0) *(f32x2*)&L.qred[w][8 * (l & 7) + 2 * (l >> 4)] = f32x2{y[0], y[1]};
  }
  __syncthreads();
  {
    f32x2 a = *(const f32x2*)&L.qred[0][2 * (t & 31)];
#pragma unroll
    for (int i = 1; i < 4; ++i) a += *(const f32x2*)&L.qred[i][2 * (t & 31)];
    PUT_PAIR(rws, t < 32 ? WS_Q + gid * 512 + t * 16 : OOB, T, a[0], a[1], POL_QH_ST);
  }
#pragma unroll
  for (int j = K0; j < K1; ++j) LD4(ak[j], rk, kvoff, j * 4096, "nt");
  PIN();
  STAMP(2);
  {
    float acc[8] = {0.f, 0.f, 0.f, 0.f, 0.f, 0.f, 0.f, 0.f};
    WAITN(2 + K1); TIE(a1[0]);
#pragma unroll
    for (int j = 1; j < 6; ++j) TIE(a1[j]);
#pragma unroll
    for (int j = 0; j < 6; ++j) fma8(acc, a1[j], x1[j]);
    if (t < 252) {
      *(f32x4*)&L.hb[r7][8 * c36] = f32x4{acc[0], acc[1], acc[2], acc[3]};
      *(f32x4*)&L.hb[r7][8 * c36 + 4] = f32x4{acc[4], acc[5], acc[6], acc[7]};
    }
  }
  __syncthreads();
  {
    const int tt = min(t, 79), j = tt / 5, c0 = 18 * j + 4 * (tt - 5 * j);
    f32x2 a = *(const f32x2*)&L.hb[0][c0], c = *(const f32x2*)&L.hb[0][c0 + 2];
#pragma unroll
    for (int i = 1; i < 7; ++i) { a += *(const f32x2*)&L.hb[i][c0]; c += *(const f32x2*)&L.hb[i][c0 + 2]; }
    PUT_QUAD(rws, t < 80 ? WS_H + gid * H_ROW + t * 16 : OOB, T, a[0], a[1], c[0], c[1], POL_QH_ST);
  }
#pragma unroll
  for (int j = K1; j < 16; ++j) LD4(ak[j], rk, kvoff, j * 4096, "nt");
  const int qo0 = WS_Q + (h * 16 + (t >> 7) * 8 + ((t & 127) >> 5)) * 512 + (t & 31) * 16, qo1 = qo0 + 4 * 512;
  u32x4 pa, pb, ph;
  LD4Z(pa, rws, qo0, POL_QH_LD); LD4Z(pb, rws, qo1, POL_QH_LD);
#pragma unroll
  for (int j = 0; j < 12; ++j) LD4(av[j], rv, vvoff, j * 4096, "nt");
  const int ho = t < 80 ? WS_H + (h * 16 + (t & 15)) * H_ROW + (s * 5 + (t >> 4)) * 16 : OOB;
  PIN();
  STAMP(3);
  WAITV2(12, pa, pb); TIE(b1v);
#pragma unroll
  for (int j = 0; j < 16; ++j) TIE(ak[j]);
  SETTLE2(rws, qo0, qo1, T, pa, pb, POL_QH_LD);
  *(f32x2*)&L.qh[t >> 7][2 * (t & 127)] = f32x2{asf(pa[0]) + asf(pb[0]), asf(pa[2]) + asf(pb[2])};
  __syncthreads();
  STAMP(4);
  u32x4 qf;
  {
    const int o = ((l >> 3) & 1) * 128 + ((l & 3) + 4 * (l >> 4)) * 8;
    const f32x4 a0 = *(const f32x4*)&L.qh[0][o] + *(const f32x4*)&L.qh[1][o];
    const f32x4 a1_ = *(const f32x4*)&L.qh[0][o + 4] + *(const f32x4*)&L.qh[1][o + 4];
    qf = u32x4{cvtpk(a0[0], a0[1]), cvtpk(a0[2], a0[3]), cvtpk(a1_[0], a1_[1]), cvtpk(a1_[2], a1_[3])};
  }
  constexpr float C2 = 0.08838834764831845f * 1.4426950408889634f;
  u32x4 a2[5], ao[4], a2x[3], aox[2];
  const int rho = l >> 4;
  const int wvo = rho * 1152 + w * 256 + (l & 15) * 16;
  const int x2o = w == 3 ? (l >> 3) * 1152 + 1024 + (l & 7) * 16 : OOB;
  float sc[16];
  {
    f32x4 d[16];
#pragma unroll
    for (int j = 0; j < 16; ++j)
      d[j] = __builtin_amdgcn_mfma_f32_16x16x32_bf16(__builtin_bit_cast(s16x8, ak[j]), __builtin_bit_cast(s16x8, qf), f32x4{0.f, 0.f, 0.f, 0.f}, 0, 0, 0);
    PIN();
#pragma unroll
    for (int j = 0; j < 16; ++j) sc[j] = (dpp<0x00>(d[j][0]) + dpp<0x55>(d[j][1])) + (dpp<0xAA>(d[j][2]) + dpp<0xFF>(d[j][3]));
  }
  float mx = -INFINITY;
#pragma unroll
  for (int j = 0; j < 16; ++j) mx = fmaxf(mx, sc[j]);
  mx = xrow_max(mx) * C2;
  float lsum = 0.f;
  unsigned pk[16];
#pragma unroll
  for (int j = 0; j < 16; ++j) { const float p = __builtin_amdgcn_exp2f(fmaf(sc[j], C2, -mx)); lsum += p; pk[j] = cvtpk(p, p); }
  lsum = xrow_sum(lsum);
  u32x4 msk;
  {
    const unsigned half = (l & 1) ? 0xffff0000u : 0x0000ffffu;
#pragma unroll
    for (int k = 0; k < 4; ++k) msk[k] = (((l & 7) >> 1) == k) ? half : 0u;
  }
  PIN();
  STAMP(5);
  f32x4 oacc = {0.f, 0.f, 0.f, 0.f};
#define PV_STEP(j, n) { const u32x4 pf = {pk[j] & msk[0], pk[j] & msk[1], pk[j] & msk[2], pk[j] & msk[3]}; WAITV1(n, av[j]); \
    oacc = __builtin_amdgcn_mfma_f32_16x16x32_bf16(__builtin_bit_cast(s16x8, pf), __builtin_bit_cast(s16x8, av[j]), oacc, 0, 0, 0); PIN(); }
  LD4(av[12], rv, vvoff, 12 * 4096, "nt"); LD4(av[13], rv, vvoff, 13 * 4096, "nt");
  LD4(av[14], rv, vvoff, 14 * 4096, "nt"); LD4(av[15], rv, vvoff, 15 * 4096, "nt");
  PV_STEP(0, 15)  LD4Z(ph, rws, ho, POL_QH_LD);
  PV_STEP(1, 15)  LD4(a2[0], rw2, wvo, 0, "nt");
  PV_STEP(2, 15)  LD4(a2[1], rw2, wvo, 4608, "nt");
  PV_STEP(3, 15)  LD4(a2[2], rw2, wvo, 2 * 4608, "nt");
  PV_STEP(4, 15)  LD4(a2[3], rw2, wvo, 3 * 4608, "nt");
  PV_STEP(5, 15)  LD4(a2[4], rw2, wvo, 4 * 4608, "nt");
  PV_STEP(6, 15)  LD4(a2x[0], rw2, x2o, 0, "nt");
  PV_STEP(7, 15)  LD4(a2x[1], rw2, x2o, 9216, "nt");
  PV_STEP(8, 15)  LD4(a2x[2], rw2, x2o, 2 * 9216, "nt");
  PV_STEP(9, 15)  LD4(ao[0], rwo, wvo, 0, "nt");
  PV_STEP(10, 15) LD4(ao[1], rwo, wvo, 4608, "nt");
  PV_STEP(11, 15) LD4(ao[2], rwo, wvo, 2 * 4608, "nt");
  PV_STEP(12, 15) LD4(ao[3], rwo, wvo, 3 * 4608, "nt");
  PV_STEP(13, 15) LD4(aox[0], rwo, x2o, 0, "nt");
  PV_STEP(14, 15) LD4(aox[1], rwo, x2o, 9216, "nt");
  PV_STEP(15, 15)
  *(f32x4*)&L.ob[w][(l >> 5) * 128 + (l & 15) * 8 + ((l >> 4) & 1) * 4] = oacc;
  if ((l & 55) == 0) *(f32x2*)&L.ml[w][l >> 3][0] = f32x2{mx, lsum};
  STAMP(6);
  __syncthreads();
  {
    const int hh = t < 64 ? t >> 5 : t & 1, oi = 4 * l;
    const f32x2 q0 = *(const f32x2*)&L.ml[0][hh][0], q1 = *(const f32x2*)&L.ml[1][hh][0], q2 = *(const f32x2*)&L.ml[2][hh][0], q3 = *(const f32x2*)&L.ml[3][hh][0];
    const f32x4 o0 = *(const f32x4*)&L.ob[0][oi], o1 = *(const f32x4*)&L.ob[1][oi], o2 = *(const f32x4*)&L.ob[2][oi], o3 = *(const f32x4*)&L.ob[3][oi];
    const float M = fmaxf(fmaxf(q0[0], q1[0]), fmaxf(q2[0], q3[0]));
    const float e0 = __builtin_amdgcn_exp2f(q0[0] - M), e1 = __builtin_amdgcn_exp2f(q1[0] - M), e2 = __builtin_amdgcn_exp2f(q2[0] - M), e3 = __builtin_amdgcn_exp2f(q3[0] - M);
    const f32x4 v = e0 * o0 + e1 * o1 + e2 * o2 + e3 * o3;
    const float ls = e0 * q0[1] + e1 * q1[1] + e2 * q2[1] + e3 * q3[1];
    const bool iso = t < 64;
    PUT_PAIR(rws, t < 66 ? WS_O + gid * O_ROW + t * 16 : OOB, T, iso ? asf(cvtpk(v[0], v[1])) : M, iso ? asf(cvtpk(v[2], v[3])) : ls, POL_O_ST);
  }
  PIN();
  STAMP(7);
  const int fin = (b & 7) * 18 + (b >> 3);
  const int ocol = (fin < 128 ? (fin >> 5) * 128 + (fin & 15) * 8 + ((fin >> 4) & 1) * 4 : 512 + (fin & 7) * 8 + ((fin >> 3) & 1) * 4) + (t & 3);
  unsigned b2v;
  { const int o = (b >> 3) < 18 ? ocol * 2 : OOB; LDHZ(b2v, rb2, o, ""); }
  WAITV1(16, ph);
  SETTLE1(rws, ho, T, t < 80, ph, 0, POL_QH_LD);
  {
    const float s0 = row_sum(blo(ph[0])), s1 = row_sum(bhi(ph[0])), s2 = row_sum(blo(ph[2])), s3 = row_sum(bhi(ph[2]));
    const float sv = (t & 2) ? ((t & 1) ? s3 : s2) : ((t & 1) ? s1 : s0);
    const float hv = rbf(rbf(sv) + blo(b1v));
    const float z = 0.7978845608028654f * (hv + 0.044715f * hv * hv * hv);
    const float th = 1.f - 2.f / (1.f + __expf(2.f * z));
    const int f = 4 * (t >> 4) + (t & 15);
    if ((t & 15) < 4 && f < 18) L.act[f] = ngate * rbf(0.5f * hv * (1.f + th));
  }
  __syncthreads();
  STAMP(12);
  float y[8] = {0.f, 0.f, 0.f, 0.f, 0.f, 0.f, 0.f, 0.f}, yx[8] = {0.f, 0.f, 0.f, 0.f, 0.f, 0.f, 0.f, 0.f};
  {
    float u[5], ux[3];
#pragma unroll
    for (int j = 0; j < 5; ++j) u[j] = L.act[4 * j + rho];
#pragma unroll
    for (int j = 0; j < 3; ++j) ux[j] = L.act[8 * j + (l >> 3)];
    WAITV1(8, a2[0]);
#pragma unroll
    for (int j = 1; j < 5; ++j) TIE(a2[j]);
#pragma unroll
    for (int j = 0; j < 3; ++j) TIE(a2x[j]);
#pragma unroll
    for (int j = 0; j < 5; ++j) fma8(y, a2[j], u[j]);
    if (w == 3) {
#pragma unroll
      for (int j = 0; j < 3; ++j) fma8(yx, a2x[j], ux[j]);
    }
#pragma unroll
    for (int k = 0; k < 8; ++k) { TIE(y[k]); TIE(yx[k]); }
  }
  PIN();
  STAMP(13);
  {
    const int ob_ = w == 0 ? WS_O + (h * 16 + (l >> 2)) * O_ROW : OOB;
    const int oo = ob_ + s * 64 + (l & 3) * 16, om = ob_ + 1024 + (s >> 3) * 16;
    const int orow = 4 * (l & 3) + rho;
    int oslot = (l & 4) ? 16 + (orow & 7) * 2 + (orow >> 3) : rho * 4 + (l & 3);
    int yo = (l & 16) == 0 ? WS_Y + b * Y_ROW + (32 * w + 16 * (l >> 5) + (l & 15)) * 16 : OOB;
    int yxo = (l & 24) == 0 ? WS_Y + b * Y_ROW + (128 + 8 * (l >> 5) + (l & 7)) * 16 : OOB;
    TIE(oslot); TIE(yo); TIE(yxo);
    u32x4 p0, m0;
    LD4Z(p0, rws, oo, POL_O_LD); LD4Z(m0, rws, om, POL_O_LD);
    WAITV1(2, ao[0]); TIE(ao[1]); TIE(ao[2]); TIE(ao[3]); TIE(aox[0]); TIE(aox[1]); TIE(b2v);
    float wf[4][8], wx[2][8];
#pragma unroll
    for (int j = 0; j < 4; ++j)
#pragma unroll
      for (int k = 0; k < 4; ++k) { wf[j][2 * k] = blo(ao[j][k]); wf[j][2 * k + 1] = bhi(ao[j][k]); TIE(wf[j][2 * k]); TIE(wf[j][2 * k + 1]); }
#pragma unroll
    for (int j = 0; j < 2; ++j)
#pragma unroll
      for (int k = 0; k < 4; ++k) { wx[j][2 * k] = blo(aox[j][k]); wx[j][2 * k + 1] = bhi(aox[j][k]); TIE(wx[j][2 * k]); TIE(wx[j][2 * k + 1]); }
    PIN();
    STAMP(14);
    WAITV2(0, p0, m0);
    STAMP(15);
    if (w == 0) {
      SETTLE2(rws, oo, om, T, p0, m0, POL_O_LD);
      STAMP(8);
      float M = asf(m0[0]);
      M = fmaxf(M, dpp<ROR4>(M)); M = fmaxf(M, dpp<ROR8>(M)); M = xrow_max(M);
      const float e = __builtin_amdgcn_exp2f(asf(m0[0]) - M);
      float Ls = e * asf(m0[2]);
      float v = fold16(fold32(e * blo(p0[0]), e * blo(p0[2])), fold32(e * bhi(p0[0]), e * bhi(p0[2])));
      Ls += dpp<ROR4>(Ls); v += dpp<ROR4>(v);
      Ls += dpp<ROR8>(Ls); v += dpp<ROR8>(v);
      Ls = xrow_sum(Ls);
      v = gate * rbf(v * __builtin_amdgcn_rcpf(Ls));
      if ((l & 8) == 0) L.ou[oslot] = v;
    }
    __syncthreads();
    const f32x4 u = *(const f32x4*)&L.ou[4 * rho];
    const f32x2 ux = *(const f32x2*)&L.ou[16 + 2 * (l >> 3)];
#pragma unroll
    for (int j = 0; j < 4; ++j)
#pragma unroll
      for (int k = 0; k < 8; ++k) y[k] = fmaf(u[j], wf[j][k], y[k]);
    float z[4];
#pragma unroll
    for (int k = 0; k < 4; ++k) z[k] = fold32(y[k], y[k + 4]);
#pragma unroll
    for (int k = 0; k < 4; ++k) z[k] = fold16(z[k], z[k]);
    PUT_QUAD(rws, yo, T, z[0], z[1], z[2], z[3], POL_Y_ST);
    if (w == 3) {
#pragma unroll
      for (int j = 0; j < 2; ++j)
#pragma unroll
        for (int k = 0; k < 8; ++k) yx[k] = fmaf(ux[j], wx[j][k], yx[k]);
#pragma unroll
      for (int k = 0; k < 4; ++k) z[k] = fold32(yx[k], yx[k + 4]);
#pragma unroll
      for (int k = 0; k < 4; ++k) { z[k] = fold16(z[k], z[k]); z[k] += dpp<ROR8>(z[k]); }
      PUT_QUAD(rws, yxo, T, z[0], z[1], z[2], z[3], POL_Y_ST);
    }
  }
  PIN();
  STAMP(9);
  if ((b >> 3) < 18) {
    const int o0 = WS_Y + t * Y_ROW + fin * 16;
    __builtin_amdgcn_s_sleep(FIN_SLEEP);
    LD4Z(pa, rws, o0, POL_Y_LD);
    WAITV1(0, pa);
    SETTLE1(rws, o0, T, true, pa, FAR_SLEEP_3, POL_Y_LD);
    STAMP(10);
    const float v = row_sum(fold16(fold32(blo(pa[0]), blo(pa[2])), fold32(bhi(pa[0]), bhi(pa[2]))));
    if ((l & 15) == 0) L.fin[w][rho] = v;
    __syncthreads();
    if (t < 4) {
      const float r = ((L.fin[0][t] + L.fin[1][t]) + (L.fin[2][t] + L.fin[3][t])) + ngate * blo(b2v);
      const unsigned pr = cvtpk(r, r); const int o = ocol * 2;
      STHZ(pr, rout, o, "");
    }
    if (b == 0 && t == 0) { const int z = WS_EPOCH; const unsigned tv = T; ST1Z(tv, rws, z, "sc1"); }
  }
  STAMP(11);
  if (t == 0) { unsigned long long* sp = (unsigned long long*)(ws + STAMP_OFF) + b * STAMP_PER_WG; for (int i = 0; i < STAMP_PER_WG; ++i) sp[i] = ts[i] ? ts[i] : ts[9]; }
}

static void kernel_launch_inner(void* const* d_in, const int* in_sizes, int n_in, void* d_out, int out_size, void* d_ws, size_t ws_size, hipStream_t stream) {
  fused_decode<<<256, 256, 0, stream>>>((const u16*)d_in[0], (const u16*)d_in[1], (const u16*)d_in[2], (const u16*)d_in[3], (const u16*)d_in[4],
                                        (const u16*)d_in[6], (const u16*)d_in[7], (const u16*)d_in[8], (const u16*)d_in[9], (const u16*)d_in[10],
                                        (unsigned char*)d_ws, (u16*)d_out);
}

__global__ void tiny(unsigned char* ws) { *(unsigned long long*)(ws + STAMP_OFF - 64) = __builtin_amdgcn_s_memrealtime(); }
__global__ void __launch_bounds__(256) probe(unsigned char* ws) {
  const unsigned long long t = __builtin_amdgcn_s_memrealtime();
  asm volatile("" ::: "v145");
  if (threadIdx.x == 0) *(unsigned long long*)(ws + STAMP_OFF - 4096 + blockIdx.x * 8) = t;
}
__global__ void spin_report(const unsigned char* ws, int n) {
  if (threadIdx.x == 0) {
    unsigned long long t0 = ~0ull;
    for (int i = 0; i < n; ++i) { const unsigned long long v = *(const unsigned long long*)(ws + STAMP_OFF - 4096 + i * 8); t0 = v < t0 ? v : t0; }
    const unsigned long long tt = *(const unsigned long long*)(ws + STAMP_OFF - 64);
    unsigned long long m = t0 - tt;
    if (m > 5000) m = 5000;
    unsigned long long st = __builtin_amdgcn_s_memrealtime();
    while (__builtin_amdgcn_s_memrealtime() - st < 100 * m) __builtin_amdgcn_s_sleep(8);
  }
}
extern "C" void kernel_launch(void* const* d_in, const int* in_sizes, int n_in, void* d_out, int out_size, void* d_ws, size_t ws_size, hipStream_t stream) {
  tiny<<<1, 1, 0, stream>>>((unsigned char*)d_ws);
  probe<<<256, 256, 0, stream>>>((unsigned char*)d_ws);
  kernel_launch_inner(d_in, in_sizes, n_in, d_out, out_size, d_ws, ws_size, stream);
  spin_report<<<1, 64, 0, stream>>>((const unsigned char*)d_ws, 256);
}
```

```cpp
#include <hip/hip_runtime.h>
#include <hip/hip_bf16.h>
#include <stdint.h>

typedef unsigned short u16;
typedef unsigned u32x4 __attribute__((ext_vector_type(4)));
typedef unsigned u32x3 __attribute__((ext_vector_type(3)));
typedef unsigned u32x2 __attribute__((ext_vector_type(2)));
typedef float f32x4 __attribute__((ext_vector_type(4)));
typedef float f32x2 __attribute__((ext_vector_type(2)));
typedef short s16x8 __attribute__((ext_vector_type(8)));

constexpr int DM = 576, HKV = 16, SK = 32768, DH = 128, WIN = 4096, FF = 4608, NQ = 4096;
constexpr int OOB = 0x40000000;

constexpr unsigned WS_EPOCH = 0;
constexpr unsigned WS_Q = 4096;
constexpr unsigned H_ROW = 1280, O_ROW = 1152, Y_ROW = 2304;
constexpr unsigned WS_H = WS_Q + 256 * 512;
constexpr unsigned WS_O = WS_H + 256 * H_ROW;
constexpr unsigned WS_Y = WS_O + 256 * O_ROW;
constexpr unsigned WS_END = WS_Y + 256 * Y_ROW;
constexpr unsigned SPIN_CAP = 1u << 18;
#define FAR_SLEEP_3 12
#define POL_Q_ST "sc1 nt"
#define POL_Q_LD "sc1 nt"
#define POL_H_ST "sc1 nt"
#define POL_O_ST "sc1"
#define POL_O_LD "sc1"
#define POL_Y_ST "sc1"
#define POL_Y_LD "sc1"
#define PRETOUCH 0
#define STAGGER 0
#define QP 0
#define VPRE 12
#define CODE_WARM 0
#define PRETOUCH_Q 0
#define ABL 0
#define PACE 0
#define PACE0 8
#define PACED(step) do { if (PACE) { const unsigned tgt_ = c0 + (unsigned)(((step) - PACE0) * PACE); unsigned long long n_; \
    do { asm volatile("s_memtime %0\n\ts_waitcnt lgkmcnt(0)" : "=s"(n_)); } while ((int)((unsigned)n_ - tgt_) < 0); } } while (0)
#define K0 12
#define K1 10
#define O_SLEEP 0
#define FIN_SLEEP 12

#define STAMP(k) do {} while (0)
#define PIN() __builtin_amdgcn_sched_barrier(0)

typedef u32x4 srd_t;
__device__ __forceinline__ srd_t mk(const void* p, unsigned bytes) {
  const unsigned long long a = (unsigned long long)p;
  return srd_t{(unsigned)a, (unsigned)(a >> 32), bytes, 0x00020000u};
}
#define LD4(dst, srd, voff, soff, pol) asm volatile("buffer_load_dwordx4 %0, %1, %2, %3 offen " pol : "=v"(dst) : "v"(voff), "s"(srd), "s"(soff))
#define LD4Z(dst, srd, voff, pol) asm volatile("buffer_load_dwordx4 %0, %1, %2, 0 offen " pol : "=v"(dst) : "v"(voff), "s"(srd))
#define LD3Z(dst, srd, voff, pol) asm volatile("buffer_load_dwordx3 %0, %1, %2, 0 offen " pol : "=v"(dst) : "v"(voff), "s"(srd))
#define LD2Z(dst, srd, voff, pol) asm volatile("buffer_load_dwordx2 %0, %1, %2, 0 offen " pol : "=v"(dst) : "v"(voff), "s"(srd))
#define LD1Z(dst, srd, voff, pol) asm volatile("buffer_load_dword %0, %1, %2, 0 offen " pol : "=v"(dst) : "v"(voff), "s"(srd))
#define LDHZ(dst, srd, voff, pol) asm volatile("buffer_load_ushort %0, %1, %2, 0 offen " pol : "=v"(dst) : "v"(voff), "s"(srd))
#define ST4Z(val, srd, voff, pol) asm volatile("buffer_store_dwordx4 %0, %1, %2, 0 offen " pol "\n\ts_nop 1" :: "v"(val), "v"(voff), "s"(srd) : "memory")
#define ST1Z(val, srd, voff, pol) asm volatile("buffer_store_dword %0, %1, %2, 0 offen " pol :: "v"(val), "v"(voff), "s"(srd) : "memory")
#define STHZ(val, srd, voff, pol) asm volatile("buffer_store_short %0, %1, %2, 0 offen " pol :: "v"(val), "v"(voff), "s"(srd) : "memory")
#define WAITV1(n, a) asm volatile("s_waitcnt vmcnt(" #n ")" : "+v"(a))
#define WAITV2(n, a, b) asm volatile("s_waitcnt vmcnt(" #n ")" : "+v"(a), "+v"(b))
#define WAITV3(n, a, b, c) asm volatile("s_waitcnt vmcnt(" #n ")" : "+v"(a), "+v"(b), "+v"(c))
#define WAITN(n) asm volatile("s_waitcnt vmcnt(%0)" :: "i"(n))
#define TIE(a) asm volatile("" : "+v"(a))
__device__ __forceinline__ float asf(unsigned u) { return __builtin_bit_cast(float, u); }
__device__ __forceinline__ unsigned asu(float f) { return __builtin_bit_cast(unsigned, f); }
__device__ __forceinline__ float blo(unsigned v) { return asf(v << 16); }
__device__ __forceinline__ float bhi(unsigned v) { return asf(v & 0xffff0000u); }
typedef __bf16 bf16x2_t __attribute__((ext_vector_type(2)));
__device__ __forceinline__ unsigned cvtpk(float lo, float hi) {
  return __builtin_bit_cast(unsigned, __builtin_convertvector(f32x2{lo, hi}, bf16x2_t));
}
__device__ __forceinline__ float rbf(float f) { return blo(cvtpk(f, f)); }
template <int CTRL> __device__ __forceinline__ float dpp(float x) {
  return asf(__builtin_amdgcn_mov_dpp((int)asu(x), CTRL, 0xf, 0xf, true));
}
constexpr int XOR1 = 0xB1, XOR2 = 0x4E, HMIR = 0x141, ROR8 = 0x128, ROR4 = 0x124;
__device__ __forceinline__ float xrow_sum(float x) {
  auto s = __builtin_amdgcn_permlane16_swap(asu(x), asu(x), false, false);
  x = asf(s[0]) + asf(s[1]);
  auto t = __builtin_amdgcn_permlane32_swap(asu(x), asu(x), false, false);
  return asf(t[0]) + asf(t[1]);
}
__device__ __forceinline__ float xrow_max(float x) {
  auto s = __builtin_amdgcn_permlane16_swap(asu(x), asu(x), false, false);
  x = fmaxf(asf(s[0]), asf(s[1]));
  auto t = __builtin_amdgcn_permlane32_swap(asu(x), asu(x), false, false);
  return fmaxf(asf(t[0]), asf(t[1]));
}
__device__ __forceinline__ float row_sum(float x) {
  x += dpp<XOR1>(x); x += dpp<XOR2>(x); x += dpp<HMIR>(x); x += dpp<ROR8>(x); return x;
}
__device__ __forceinline__ float row_max(float x) {
  x = fmaxf(x, dpp<XOR1>(x)); x = fmaxf(x, dpp<XOR2>(x)); x = fmaxf(x, dpp<HMIR>(x)); x = fmaxf(x, dpp<ROR8>(x)); return x;
}
__device__ __forceinline__ void fma8(float (&acc)[8], u32x4 w, float x) {
#pragma unroll
  for (int k = 0; k < 4; ++k) { acc[2 * k] = fmaf(x, blo(w[k]), acc[2 * k]); acc[2 * k + 1] = fmaf(x, bhi(w[k]), acc[2 * k + 1]); }
}
#define PUT_QUAD(r, voff, T, a, b, c, d, pol) PUT_PAIR(r, voff, T, asf(cvtpk(a, b)), asf(cvtpk(c, d)), pol)
#define PUT_PAIR(r, voff, T, a, b, pol) do { const float a_ = (a), b_ = (b); const int o_ = (voff); \
    const u32x4 v_ = {asu(a_), (T) ^ asu(b_), asu(b_), (T) ^ asu(a_)}; ST4Z(v_, r, o_, pol); } while (0)
__device__ __forceinline__ bool pair_ok(u32x4 v, unsigned T) { return ((v[1] ^ v[2]) == T) & ((v[3] ^ v[0]) == T); }
typedef unsigned long long u64;
#define BALLOT(c) __builtin_amdgcn_ballot_w64(c)
#define SETTLE1(r, o0, T, live, a, far_sleep, pol) \
  _Pragma("unroll 1") for (unsigned spins_ = 0; spins_ < SPIN_CAP; ++spins_) { \
    const u64 ma_ = BALLOT((live) & !pair_ok(a, T)); \
    if (ma_ == 0) break; \
    if (far_sleep && __popcll(ma_) > 16) __builtin_amdgcn_s_sleep(far_sleep); \
    u64 sv_; \
    asm volatile("s_mov_b64 %1, exec\n\ts_mov_b64 exec, %2\n\tbuffer_load_dwordx4 %0, %3, %4, 0 offen " pol "\n\ts_mov_b64 exec, %1\n\ts_waitcnt vmcnt(0)" \
                 : "+v"(a), "=&s"(sv_) : "s"(ma_), "v"(o0), "s"(r) : "memory"); }
#define RETRY_SLEEP 0
#define SETTLE2S(r, o0, o1, T, a, b, slp, pol) SETTLE2G(r, o0, o1, T, a, b, slp, -1, pol)
#define SETTLE2(r, o0, o1, T, a, b, pol) SETTLE2G(r, o0, o1, T, a, b, 0, 0, pol)
#define SETTLE2G(r, o0, o1, T, a, b, far_sleep, thr, pol) \
  _Pragma("unroll 1") for (unsigned spins_ = 0; spins_ < SPIN_CAP; ++spins_) { \
    const u64 ma_ = BALLOT(!pair_ok(a, T)), mb_ = BALLOT(!pair_ok(b, T)); \
    if ((ma_ | mb_) == 0) break; \
    if (far_sleep && (int)(__popcll(ma_) + __popcll(mb_)) > (thr)) __builtin_amdgcn_s_sleep(far_sleep); \
    u64 sv_; \
    asm volatile("s_mov_b64 %2, exec\n\ts_mov_b64 exec, %3\n\tbuffer_load_dwordx4 %0, %5, %7, 0 offen " pol "\n\t" \
                 "s_mov_b64 exec, %4\n\tbuffer_load_dwordx4 %1, %6, %7, 0 offen " pol "\n\ts_mov_b64 exec, %2\n\ts_waitcnt vmcnt(0)" \
                 : "+v"(a), "+v"(b), "=&s"(sv_) : "s"(ma_), "s"(mb_), "v"(o0), "v"(o1), "s"(r) : "memory"); }
#define SETTLE4(r, o0, o1, o2, o3, T, a, b, c, d, pol) \
  _Pragma("unroll 1") for (unsigned spins_ = 0; spins_ < SPIN_CAP; ++spins_) { \
    const u64 m0_ = BALLOT(!(pair_ok(a, T) & pair_ok(b, T))), m1_ = BALLOT(!(pair_ok(c, T) & pair_ok(d, T))); \
    if ((m0_ | m1_) == 0) break; \
    u64 sv_; \
    asm volatile("s_mov_b64 %4, exec\n\ts_mov_b64 exec, %5\n\tbuffer_load_dwordx4 %0, %7, %11, 0 offen " pol "\n\tbuffer_load_dwordx4 %1, %8, %11, 0 offen " pol "\n\t" \
                 "s_mov_b64 exec, %6\n\tbuffer_load_dwordx4 %2, %9, %11, 0 offen " pol "\n\tbuffer_load_dwordx4 %3, %10, %11, 0 offen " pol "\n\t" \
                 "s_mov_b64 exec, %4\n\ts_waitcnt vmcnt(0)" \
                 : "+v"(a), "+v"(b), "+v"(c), "+v"(d), "=&s"(sv_) : "s"(m0_), "s"(m1_), "v"(o0), "v"(o1), "v"(o2), "v"(o3), "s"(r) : "memory"); }
__device__ __forceinline__ f32x4 rowmix(unsigned u2, u32x4 msk, u32x4 wreg, f32x4 acc) {
  const u32x4 a = {u2 & msk[0], u2 & msk[1], u2 & msk[2], u2 & msk[3]};
  return __builtin_amdgcn_mfma_f32_16x16x32_bf16(__builtin_bit_cast(s16x8, a), __builtin_bit_cast(s16x8, wreg), acc, 0, 0, 0);
}
__device__ __forceinline__ float fold32(float a, float b) {
  auto s = __builtin_amdgcn_permlane32_swap(asu(a), asu(b), false, false);
  return asf(s[0]) + asf(s[1]);
}
__device__ __forceinline__ float fold16(float a, float b) {
  auto s = __builtin_amdgcn_permlane16_swap(asu(a), asu(b), false, false);
  return asf(s[0]) + asf(s[1]);
}
struct Lds {
  float qred[8][64];
  float qh[2][256];
  float hb[6][292];
  float ob[4][256];
  float ml[4][2][2];
  unsigned um[4][12];
  unsigned ux[8][6];
  float fin[4][4];
  float gt[2];
};

__global__ void __launch_bounds__(256, 2)
fused_decode(const u16* __restrict__ x, const u16* __restrict__ kc, const u16* __restrict__ vc, const u16* __restrict__ wq,
             const u16* __restrict__ wo, const u16* __restrict__ w1, const u16* __restrict__ b1, const u16* __restrict__ w2,
             const u16* __restrict__ b2, const u16* __restrict__ gw, unsigned char* ws, u16* out) {
  __shared__ __attribute__((aligned(16))) Lds L;
  STAMP(0);
  unsigned c0 = 0;
  if (PACE) { unsigned long long n_; asm volatile("s_memtime %0\n\ts_waitcnt lgkmcnt(0)" : "=s"(n_)); c0 = (unsigned)n_; }
  const int b = blockIdx.x, t = threadIdx.x, l = t & 63, w = __builtin_amdgcn_readfirstlane(t >> 6);
  const int h = ((b & 7) << 1) | ((b >> 3) & 1), s = b >> 4, gid = h * 16 + s;
#define XS 4
  const int hc = (h + 2 * XS) & 15, gidc = hc * 16 + s;
  const int cb = s & 3, rg = s >> 2;

  const srd_t rws = mk(ws, WS_END);
  const srd_t rx = mk(x, DM * 2), rgw = mk(gw, DM * 2);
#define XQ 4
  const int hq = (h + 2 * XQ) & 15;
  const srd_t rwq = mk(wq + (size_t)(144 * rg) * NQ + hq * 256 + cb * 64, 143 * 8192 + 128);
  const size_t kvo = ((size_t)h * SK + (SK - WIN) + s * 256) * DH;
  const srd_t rk = mk(kc + kvo, 65536), rv = mk(vc + kvo, 65536);
  const srd_t rw1 = mk(w1 + (size_t)(36 * s) * FF + h * 288, 35 * 9216 + 576);
  const srd_t rw2 = mk(w2 + (size_t)gidc * 18 * DM, 18 * 1152);
  const srd_t rwo = mk(wo + (size_t)gidc * 16 * DM, 16 * 1152);
  const srd_t rb1 = mk(b1 + gidc * 18, 36);

  const srd_t rb2 = mk(b2, DM * 2), rout = mk(out, DM * 2);
  unsigned epoch, xq4, ptq; u32x2 xq; u32x4 aq[5];
  {
    const int g = l >> 3, xo = (144 * rg + 36 * w) * 2;
    const int o0 = xo + 8 * g, o1 = g < 4 ? xo + 64 + 2 * g : OOB, z = WS_EPOCH;
    const int vo = (36 * w + 4 * g) * 8192 + (l & 7) * 16, v4 = g < 4 ? (36 * w + 32 + g) * 8192 + (l & 7) * 16 : OOB;
#pragma unroll
    for (int j = 0; j < 4; ++j) LD4(aq[j], rwq, vo, j * 8192, "nt");
    LD4Z(aq[4], rwq, v4, "nt");
    LD2Z(xq, rx, o0, ""); LDHZ(xq4, rx, o1, "");
    LD1Z(epoch, rws, z, "sc1");
    const int pq = PRETOUCH_Q && t < 4 ? WS_Q + (hq * 16 + s) * 512 + t * 128 : OOB;
    LD1Z(ptq, rws, pq, "");
  }
  u32x4 ak[16], av[16];
  const int kvoff = w * 1024 + ((l & 15) >> 2) * 256 + (l & 3) * 16 + (l >> 4) * 64;
  const int vvoff = w * 1024 + l * 16;
  if (STAGGER) __builtin_amdgcn_s_sleep(STAGGER);
#pragma unroll
  for (int j = 0; j < K0; ++j) { if (j + 5 > PACE0) PACED(j + 5); LD4(ak[j], rk, kvoff, j * 4096, "nt"); }
  u32x4 msk;
  {
    const unsigned half = (l & 1) ? 0xffff0000u : 0x0000ffffu;
#pragma unroll
    for (int k = 0; k < 4; ++k) { msk[k] = (((l & 7) >> 1) == k) ? half : 0u; TIE(msk[k]); }
  }
  if (t >= 128 && t < 128 + 96) (&L.um[0][0])[t - 128] = 0u;
  int qpo = t < 32 ? WS_Q + (hq * 16 + s) * 512 + t * 16 : OOB;
  TIE(qpo);
  PIN();
  STAMP(12);
  WAITN(K0); TIE(ptq); TIE(epoch); TIE(xq); TIE(xq4); TIE(aq[0]); TIE(aq[1]); TIE(aq[2]); TIE(aq[3]); TIE(aq[4]);
  STAMP(1);
  unsigned T = __builtin_amdgcn_readfirstlane(epoch) + 1u;
  T = T ? T : 1u;
  {
    f32x4 acc = {0.f, 0.f, 0.f, 0.f};
    acc = rowmix(__builtin_amdgcn_perm(xq[0], xq[0], 0x01000100u), msk, aq[0], acc);
    acc = rowmix(__builtin_amdgcn_perm(xq[0], xq[0], 0x03020302u), msk, aq[1], acc);
    acc = rowmix(__builtin_amdgcn_perm(xq[1], xq[1], 0x01000100u), msk, aq[2], acc);
    acc = rowmix(__builtin_amdgcn_perm(xq[1], xq[1], 0x03020302u), msk, aq[3], acc);
    acc = rowmix(__builtin_amdgcn_perm(xq4, xq4, 0x01000100u), msk, aq[4], acc);
    if ((l >> 5) == ((l >> 3) & 1)) *(f32x4*)&L.qred[2 * w + (l >> 5)][8 * (l & 7) + 4 * ((l >> 4) & 1)] = acc;
  }
  __syncthreads();
  {
    f32x2 a = *(const f32x2*)&L.qred[0][2 * (t & 31)];
#pragma unroll
    for (int i = 1; i < 8; ++i) a += *(const f32x2*)&L.qred[i][2 * (t & 31)];
    PUT_PAIR(rws, qpo, T, a[0], a[1], POL_Q_ST);
  }
  PIN();
  STAMP(2);
#pragma unroll
  for (int j = K0; j < 16; ++j) { PACED(j + 5); LD4(ak[j], rk, kvoff, j * 4096, "nt"); }
  u32x4 a1[6]; u32x3 x1; unsigned b1v, pt;
  const int r6 = t / 36, c36 = t - r6 * 36;
  {
    const int vo = t < 216 ? r6 * 6 * 9216 + c36 * 16 : OOB, xo = (36 * s + 6 * min(r6, 5)) * 2;
    const int bo = t >= 64 ? (((t - 64) >> 4) * 4 + (t & 3)) * 2 : OOB;
    const int po = PRETOUCH && t < 9 ? WS_O + gid * O_ROW + t * 128 : OOB;
    LD3Z(x1, rx, xo, ""); LDHZ(b1v, rb1, bo, ""); LD1Z(pt, rws, po, "");
#pragma unroll
    for (int j = 0; j < 6; ++j) { PACED(21 + (j * 5) / 6); LD4(a1[j], rw1, vo, j * 9216, "nt"); }
  }
  const int qo0 = WS_Q + (h * 16 + (t >> 7) * 8 + ((t & 127) >> 5)) * 512 + (t & 31) * 16, qo1 = qo0 + 4 * 512;
  u32x4 pa, pb;
#pragma unroll
  for (int j = 0; j < QP; ++j) LD4(av[j], rv, vvoff, j * 4096, "nt");
  LD4Z(pa, rws, qo0, POL_Q_LD); LD4Z(pb, rws, qo1, POL_Q_LD);
#pragma unroll
  for (int j = QP; j < VPRE; ++j) { PACED(26 + j); LD4(av[j], rv, vvoff, j * 4096, "nt"); }
  PIN();
  STAMP(3);
  {
    float acc[8] = {0.f, 0.f, 0.f, 0.f, 0.f, 0.f, 0.f, 0.f};
    WAITN(VPRE + 2); TIE(x1); TIE(b1v); TIE(pt);
#pragma unroll
    for (int j = 0; j < 6; ++j) TIE(a1[j]);
#pragma unroll
    for (int j = 0; j < 16; ++j) TIE(ak[j]);
#pragma unroll
    for (int j = 0; j < 6; ++j) fma8(acc, a1[j], (j & 1) ? bhi(x1[j >> 1]) : blo(x1[j >> 1]));
    if (t < 216) {
      *(f32x4*)&L.hb[r6][8 * c36] = f32x4{acc[0], acc[1], acc[2], acc[3]};
      *(f32x4*)&L.hb[r6][8 * c36 + 4] = f32x4{acc[4], acc[5], acc[6], acc[7]};
    }
  }
  PIN();
  STAMP(15);
  WAITN(VPRE - QP); TIE(pa); TIE(pb);
  SETTLE2(rws, qo0, qo1, T, pa, pb, POL_Q_LD);
  *(f32x2*)&L.qh[t >> 7][2 * (t & 127)] = f32x2{asf(pa[0]) + asf(pb[0]), asf(pa[2]) + asf(pb[2])};
  __syncthreads();
  STAMP(4);
  u32x4 qf;
  {
    const int o = ((l >> 3) & 1) * 128 + ((l & 3) + 4 * (l >> 4)) * 8;
    const f32x4 a0 = *(const f32x4*)&L.qh[0][o] + *(const f32x4*)&L.qh[1][o];
    const f32x4 a1_ = *(const f32x4*)&L.qh[0][o + 4] + *(const f32x4*)&L.qh[1][o + 4];
    qf = u32x4{cvtpk(a0[0], a0[1]), cvtpk(a0[2], a0[3]), cvtpk(a1_[0], a1_[1]), cvtpk(a1_[2], a1_[3])};
  }
  constexpr float C2 = 0.08838834764831845f * 1.4426950408889634f;
  u32x4 a2[5], ao[4], a2x[3], aox[2];
  const int rho = l >> 4;
  const int wvo = rho * 1152 + w * 256 + (l & 15) * 16;
  const int x2o = w == 3 ? (l >> 3) * 1152 + 1024 + (l & 7) * 16 : OOB;
  float sc[16];
  {
    f32x4 d[16];
#pragma unroll
    for (int j = 0; j < 16; ++j)
      d[j] = __builtin_amdgcn_mfma_f32_16x16x32_bf16(__builtin_bit_cast(s16x8, ak[j]), __builtin_bit_cast(s16x8, qf), f32x4{0.f, 0.f, 0.f, 0.f}, 0, 0, 0);
    PIN();
#pragma unroll
    for (int j = VPRE; j < (VPRE + 16) / 2; ++j) LD4(av[j], rv, vvoff, j * 4096, "nt");
    PIN();
#pragma unroll
    for (int j = 0; j < 16; ++j) sc[j] = (dpp<0x00>(d[j][0]) + dpp<0x55>(d[j][1])) + (dpp<0xAA>(d[j][2]) + dpp<0xFF>(d[j][3]));
  }
  PIN();
#pragma unroll
  for (int j = (VPRE + 16) / 2; j < 16; ++j) LD4(av[j], rv, vvoff, j * 4096, "nt");
  PIN();
  float mx = -INFINITY;
#pragma unroll
  for (int j = 0; j < 16; ++j) mx = fmaxf(mx, sc[j]);
  mx = xrow_max(mx) * C2;
  float lsum = 0.f;
  unsigned pk[16];
#pragma unroll
  for (int j = 0; j < 16; ++j) { const float p = __builtin_amdgcn_exp2f(fmaf(sc[j], C2, -mx)); lsum += p; pk[j] = cvtpk(p, p); }
  lsum = xrow_sum(lsum);
  PIN();
  STAMP(5);
  f32x4 oacc = {0.f, 0.f, 0.f, 0.f};
  {
    const int tt = min(t, 79), j = tt / 5, c0 = 18 * j + 4 * (tt - 5 * j);
    f32x2 a = *(const f32x2*)&L.hb[0][c0], c = *(const f32x2*)&L.hb[0][c0 + 2];
#pragma unroll
    for (int i = 1; i < 6; ++i) { a += *(const f32x2*)&L.hb[i][c0]; c += *(const f32x2*)&L.hb[i][c0 + 2]; }
    PUT_QUAD(rws, t < 80 ? WS_H + gid * H_ROW + t * 16 : OOB, T, a[0], a[1], c[0], c[1], POL_H_ST);
  }
  PIN();
  u32x4 xa, xb, ga, gb; unsigned b2v;
  const int go0 = w == 3 ? l * 16 : OOB, go1 = w == 3 ? l * 16 + 1024 : OOB;
  const int fin = (b & 7) * 18 + (b >> 3);
  const int ocol = (fin < 128 ? (fin >> 5) * 128 + (fin & 15) * 8 + ((fin >> 4) & 1) * 4 : 512 + (fin & 7) * 8 + ((fin >> 3) & 1) * 4) + (t & 3);
  const int b2o = (b >> 3) < 18 ? ocol * 2 : OOB;
  int opo = WS_O + gid * O_ROW + l * 16, mpo = (l & 31) == 0 ? WS_O + gid * O_ROW + 1024 + (l >> 5) * 16 : OOB;
  if (ABL & 1) { opo = OOB; mpo = OOB; }
  TIE(opo); TIE(mpo);
#define PV_STEP(j, n) { WAITV1(n, av[j]); oacc = rowmix(pk[j], msk, av[j], oacc); PIN(); }
  PV_STEP(0, 16)  LD4(a2[0], rw2, wvo, 0, "nt");
  PV_STEP(1, 16)  LD4(a2[1], rw2, wvo, 4608, "nt");
  PV_STEP(2, 16)  LD4(a2[2], rw2, wvo, 2 * 4608, "nt");
  PV_STEP(3, 16)  LD4(a2[3], rw2, wvo, 3 * 4608, "nt");
  PV_STEP(4, 16)  LD4(a2[4], rw2, wvo, 4 * 4608, "nt");
  PV_STEP(5, 16)  LD4(a2x[0], rw2, x2o, 0, "nt");
  PV_STEP(6, 16)  LD4(a2x[1], rw2, x2o, 9216, "nt");
  PV_STEP(7, 16)  LD4(a2x[2], rw2, x2o, 2 * 9216, "nt");
  PV_STEP(8, 16)  LD4(ao[0], rwo, wvo, 0, "nt");
  PV_STEP(9, 16)  LD4(ao[1], rwo, wvo, 4608, "nt");
  PV_STEP(10, 16) LD4(ao[2], rwo, wvo, 2 * 4608, "nt");
  PV_STEP(11, 16) LD4(ao[3], rwo, wvo, 3 * 4608, "nt");
  PV_STEP(12, 16) LD4(aox[0], rwo, x2o, 0, "nt");
  PV_STEP(13, 16) LD4(aox[1], rwo, x2o, 9216, "nt");
  PV_STEP(14, 16) LD4Z(xa, rx, go0, ""); LD4Z(xb, rx, go1, ""); LD4Z(ga, rgw, go0, ""); LD4Z(gb, rgw, go1, ""); LDHZ(b2v, rb2, b2o, "");
  PV_STEP(15, 20)
  *(f32x4*)&L.ob[w][(l >> 5) * 128 + (l & 15) * 8 + ((l >> 4) & 1) * 4] = oacc;
  if ((l & 55) == 0) *(f32x2*)&L.ml[w][l >> 3][0] = f32x2{mx, lsum};
  STAMP(6);
  __syncthreads();
  if (w == 3) {
    const int hh = l >> 5, oi = 4 * l;
    const f32x2 q0 = *(const f32x2*)&L.ml[0][hh][0], q1 = *(const f32x2*)&L.ml[1][hh][0], q2 = *(const f32x2*)&L.ml[2][hh][0], q3 = *(const f32x2*)&L.ml[3][hh][0];
    const f32x4 o0 = *(const f32x4*)&L.ob[0][oi], o1 = *(const f32x4*)&L.ob[1][oi], o2 = *(const f32x4*)&L.ob[2][oi], o3 = *(const f32x4*)&L.ob[3][oi];
    const float M = fmaxf(fmaxf(q0[0], q1[0]), fmaxf(q2[0], q3[0]));
    const float e0 = __builtin_amdgcn_exp2f(q0[0] - M), e1 = __builtin_amdgcn_exp2f(q1[0] - M), e2 = __builtin_amdgcn_exp2f(q2[0] - M), e3 = __builtin_amdgcn_exp2f(q3[0] - M);
    const f32x4 v = e0 * o0 + e1 * o1 + e2 * o2 + e3 * o3;
    PUT_QUAD(rws, opo, T, v[0], v[1], v[2], v[3], POL_O_ST);
    PUT_PAIR(rws, mpo, T, M, e0 * q0[1] + e1 * q1[1] + e2 * q2[1] + e3 * q3[1], POL_O_ST);
  }
  PIN();
  STAMP(7);
  float ngate;
  {
    const int th_ = t - 64;
    const int ob_ = (ABL & 2) ? OOB : WS_O + (hc * 16 + (l >> 2)) * O_ROW;
    const bool hl = th_ >= 0 && th_ < 80;
    const int oo = w == 0 ? ob_ + s * 64 + (l & 3) * 16 : hl ? WS_H + (hc * 16 + (t & 15)) * H_ROW + (s * 5 + (th_ >> 4)) * 16 : OOB;
    const int om = w == 0 ? ob_ + 1024 + (s >> 3) * 16 : OOB;
    const int row = w == 0 ? 4 * (l & 3) + rho : 4 * (th_ >> 4) + (l & 3);
    int slot = (l & 4) ? 48 + (row & 7) * 6 + (row >> 3) + (w == 0 ? 0 : 2) : (row & 3) * 12 + (row >> 2) + (w == 0 ? 0 : 4);
    const bool wr = (l & 8) == 0 && (w == 0 || (hl && row < 18));
    int yo = l < 32 ? WS_Y + b * Y_ROW + (32 * w + l) * 16 : OOB;
    int yxo = (l & 40) == 0 ? WS_Y + b * Y_ROW + (128 + 8 * rho + (l & 7)) * 16 : OOB;
    TIE(slot); TIE(yo); TIE(yxo);
    u32x4 p0, m0;
    if (w < 3) {
      WAITN(11);
      if (O_SLEEP) __builtin_amdgcn_s_sleep(O_SLEEP);
      STAMP(13);
      LD4Z(p0, rws, oo, POL_O_LD); LD4Z(m0, rws, om, POL_O_LD);
      WAITN(2);
      STAMP(14);
      WAITV2(0, p0, m0);
    } else {
      WAITN(2);
      p0 = m0 = u32x4{0u, 0u, 0u, 0u};
    }
    TIE(b2v); TIE(xa); TIE(xb); TIE(ga); TIE(gb);
#pragma unroll
    for (int j = 0; j < 5; ++j) TIE(a2[j]);
#pragma unroll
    for (int j = 0; j < 4; ++j) TIE(ao[j]);
    TIE(a2x[0]); TIE(a2x[1]); TIE(a2x[2]); TIE(aox[0]); TIE(aox[1]);
    float v = 0.f;
    if (w == 0) {
      if (!(ABL & 4)) SETTLE2S(rws, oo, om, T, p0, m0, RETRY_SLEEP, POL_O_LD);
      STAMP(8);
      float M = asf(m0[0]);
      M = fmaxf(M, dpp<ROR4>(M)); M = fmaxf(M, dpp<ROR8>(M)); M = xrow_max(M);
      const float e = __builtin_amdgcn_exp2f(asf(m0[0]) - M);
      float Ls = e * asf(m0[2]);
      v = fold16(fold32(e * blo(p0[0]), e * blo(p0[2])), fold32(e * bhi(p0[0]), e * bhi(p0[2])));
      Ls += dpp<ROR4>(Ls); v += dpp<ROR4>(v);
      Ls += dpp<ROR8>(Ls); v += dpp<ROR8>(v);
      Ls = xrow_sum(Ls);
      v *= __builtin_amdgcn_rcpf(Ls);
    } else if (w < 3) {
      SETTLE1(rws, oo, T, hl, p0, 0, POL_O_LD);
      const float s0 = row_sum(blo(p0[0])), s1 = row_sum(bhi(p0[0])), s2 = row_sum(blo(p0[2])), s3 = row_sum(bhi(p0[2]));
      const float sv = (t & 2) ? ((t & 1) ? s3 : s2) : ((t & 1) ? s1 : s0);
      const float hv = rbf(rbf(sv) + blo(b1v));
      const float z = 0.7978845608028654f * (hv + 0.044715f * hv * hv * hv);
      const float th = 1.f - 2.f * __builtin_amdgcn_rcpf(1.f + __expf(2.f * z));
      v = 0.5f * hv * (1.f + th);
    } else {
      float g = 0.f;
#pragma unroll
      for (int k = 0; k < 4; ++k) {
        const unsigned q0 = cvtpk(blo(xa[k]) * blo(ga[k]), bhi(xa[k]) * bhi(ga[k])), q1 = cvtpk(blo(xb[k]) * blo(gb[k]), bhi(xb[k]) * bhi(gb[k]));
        g += (blo(q0) + bhi(q0)) + (blo(q1) + bhi(q1));
      }
      g = rbf(xrow_sum(row_sum(g)));
      g = rbf(__builtin_amdgcn_rcpf(1.f + __expf(-g)));
      if (l == 0) *(f32x2*)L.gt = f32x2{g, rbf(1.f - g)};
    }
    if (wr) (&L.um[0][0])[slot] = cvtpk(v, v);
    __syncthreads();
    const u32x4 uo = *(const u32x4*)&L.um[rho][0], ua = *(const u32x4*)&L.um[rho][4];
    const unsigned ua4 = L.um[rho][8];
    const f32x2 gt = *(const f32x2*)L.gt;
    const float gate = gt[0];
    ngate = gt[1];
    f32x4 ya = {0.f, 0.f, 0.f, 0.f}, yb = {0.f, 0.f, 0.f, 0.f};
#pragma unroll
    for (int j = 0; j < 4; ++j) { ya = rowmix(uo[j], msk, ao[j], ya); yb = rowmix(ua[j], msk, a2[j], yb); }
    yb = rowmix(ua4, msk, a2[4], yb);
    const f32x4 y = gate * ya + ngate * yb;
    PUT_QUAD(rws, yo, T, y[0], y[1], y[2], y[3], POL_Y_ST);
    if (w == 3) {
      const unsigned* ux = &L.ux[l >> 3][0];
      f32x4 xa = {0.f, 0.f, 0.f, 0.f}, xb = {0.f, 0.f, 0.f, 0.f};
      xa = rowmix(ux[0], msk, aox[0], xa); xb = rowmix(ux[2], msk, a2x[0], xb);
      xa = rowmix(ux[1], msk, aox[1], xa); xb = rowmix(ux[3], msk, a2x[1], xb);
      xb = rowmix(ux[4], msk, a2x[2], xb);
      f32x4 z = gate * xa + ngate * xb;
#pragma unroll
      for (int k = 0; k < 4; ++k) {
        const float r = asf(__builtin_amdgcn_update_dpp((int)asu(z[k]), (int)asu(z[k]), ROR8, 0xC, 0xF, false));
        z[k] = fold32(r, r);
      }
      PUT_QUAD(rws, yxo, T, z[0], z[1], z[2], z[3], POL_Y_ST);
    }
  }
  PIN();
  STAMP(9);
  if ((b >> 3) < 18) {
    const int o0 = WS_Y + t * Y_ROW + fin * 16;
    __builtin_amdgcn_s_sleep(FIN_SLEEP);
    LD4Z(pa, rws, o0, POL_Y_LD);
    WAITV1(0, pa);
    SETTLE1(rws, o0, T, true, pa, FAR_SLEEP_3, POL_Y_LD);
    STAMP(10);
    const float v = row_sum(fold16(fold32(blo(pa[0]), blo(pa[2])), fold32(bhi(pa[0]), bhi(pa[2]))));
    if ((l & 15) == 0) L.fin[w][rho] = v;
    __syncthreads();
    if (t < 4) {
      const float r = ((L.fin[0][t] + L.fin[1][t]) + (L.fin[2][t] + L.fin[3][t])) + ngate * blo(b2v);
      const unsigned pr = cvtpk(r, r); const int o = ocol * 2;
      STHZ(pr, rout, o, "");
    }
    if (b == 0 && t == 0) { const int z = WS_EPOCH; const unsigned tv = T; ST1Z(tv, rws, z, "sc1"); }
  }
}

extern "C" void kernel_launch(void* const* d_in, const int* in_sizes, int n_in, void* d_out, int out_size, void* d_ws, size_t ws_size, hipStream_t stream) {
  fused_decode<<<256, 256, 0, stream>>>((const u16*)d_in[0], (const u16*)d_in[1], (const u16*)d_in[2], (const u16*)d_in[3], (const u16*)d_in[4],
                                        (const u16*)d_in[6], (const u16*)d_in[7], (const u16*)d_in[8], (const u16*)d_in[9], (const u16*)d_in[10],
                                        (unsigned char*)d_ws, (u16*)d_out);
}
```

```cpp
#define STAMPS
#include <hip/hip_runtime.h>
#include <hip/hip_bf16.h>
#include <stdint.h>

typedef unsigned short u16;
typedef unsigned u32x4 __attribute__((ext_vector_type(4)));
typedef unsigned u32x3 __attribute__((ext_vector_type(3)));
typedef unsigned u32x2 __attribute__((ext_vector_type(2)));
typedef float f32x4 __attribute__((ext_vector_type(4)));
typedef float f32x2 __attribute__((ext_vector_type(2)));
typedef short s16x8 __attribute__((ext_vector_type(8)));

constexpr int DM = 576, HKV = 16, SK = 32768, DH = 128, WIN = 4096, FF = 4608, NQ = 4096;
constexpr int OOB = 0x40000000;

constexpr unsigned WS_EPOCH = 0;
constexpr unsigned WS_Q = 4096;
constexpr unsigned H_ROW = 1280, O_ROW = 1152, Y_ROW = 2304;
constexpr unsigned WS_H = WS_Q + 256 * 512;
constexpr unsigned WS_O = WS_H + 256 * H_ROW;
constexpr unsigned WS_Y = WS_O + 256 * O_ROW;
constexpr unsigned WS_END = WS_Y + 256 * Y_ROW;
constexpr unsigned SPIN_CAP = 1u << 18;
#define FAR_SLEEP_3 12
#define POL_Q_ST "sc1 nt"
#define POL_Q_LD "sc1 nt"
#define POL_H_ST "sc1 nt"
#define POL_O_ST "sc1"
#define POL_O_LD "sc1"
#define POL_Y_ST "sc1"
#define POL_Y_LD "sc1"
#define PRETOUCH 0
#define STAGGER 0
#define QP 0
#define VPRE 12
#define CODE_WARM 0
#define PRETOUCH_Q 0
#define ABL 0
#define PACE 0
#define PACE0 8
#define PACED(step) do { if (PACE) { const unsigned tgt_ = c0 + (unsigned)(((step) - PACE0) * PACE); unsigned long long n_; \
    do { asm volatile("s_memtime %0\n\ts_waitcnt lgkmcnt(0)" : "=s"(n_)); } while ((int)((unsigned)n_ - tgt_) < 0); } } while (0)
#define K0 12
#define K1 10
#define O_SLEEP 0
#define FIN_SLEEP 12

#define STAMP_PER_WG 16
#define STAMP_WORDS (256 * STAMP_PER_WG)
#define STAMP_OFF (64u << 20)
#define STAMP(k) do { __builtin_amdgcn_sched_barrier(0); asm volatile("s_memrealtime %0\n\ts_waitcnt lgkmcnt(0)" : "=s"(ts[k]) :: "memory"); __builtin_amdgcn_sched_barrier(0); } while (0)
#define PIN() __builtin_amdgcn_sched_barrier(0)

typedef u32x4 srd_t;
__device__ __forceinline__ srd_t mk(const void* p, unsigned bytes) {
  const unsigned long long a = (unsigned long long)p;
  return srd_t{(unsigned)a, (unsigned)(a >> 32), bytes, 0x00020000u};
}
#define LD4(dst, srd, voff, soff, pol) asm volatile("buffer_load_dwordx4 %0, %1, %2, %3 offen " pol : "=v"(dst) : "v"(voff), "s"(srd), "s"(soff))
#define LD4Z(dst, srd, voff, pol) asm volatile("buffer_load_dwordx4 %0, %1, %2, 0 offen " pol : "=v"(dst) : "v"(voff), "s"(srd))
#define LD3Z(dst, srd, voff, pol) asm volatile("buffer_load_dwordx3 %0, %1, %2, 0 offen " pol : "=v"(dst) : "v"(voff), "s"(srd))
#define LD2Z(dst, srd, voff, pol) asm volatile("buffer_load_dwordx2 %0, %1, %2, 0 offen " pol : "=v"(dst) : "v"(voff), "s"(srd))
#define LD1Z(dst, srd, voff, pol) asm volatile("buffer_load_dword %0, %1, %2, 0 offen " pol : "=v"(dst) : "v"(voff), "s"(srd))
#define LDHZ(dst, srd, voff, pol) asm volatile("buffer_load_ushort %0, %1, %2, 0 offen " pol : "=v"(dst) : "v"(voff), "s"(srd))
#define ST4Z(val, srd, voff, pol) asm volatile("buffer_store_dwordx4 %0, %1, %2, 0 offen " pol "\n\ts_nop 1" :: "v"(val), "v"(voff), "s"(srd) : "memory")
#define ST1Z(val, srd, voff, pol) asm volatile("buffer_store_dword %0, %1, %2, 0 offen " pol :: "v"(val), "v"(voff), "s"(srd) : "memory")
#define STHZ(val, srd, voff, pol) asm volatile("buffer_store_short %0, %1, %2, 0 offen " pol :: "v"(val), "v"(voff), "s"(srd) : "memory")
#define WAITV1(n, a) asm volatile("s_waitcnt vmcnt(" #n ")" : "+v"(a))
#define WAITV2(n, a, b) asm volatile("s_waitcnt vmcnt(" #n ")" : "+v"(a), "+v"(b))
#define WAITV3(n, a, b, c) asm volatile("s_waitcnt vmcnt(" #n ")" : "+v"(a), "+v"(b), "+v"(c))
#define WAITN(n) asm volatile("s_waitcnt vmcnt(%0)" :: "i"(n))
#define TIE(a) asm volatile("" : "+v"(a))
__device__ __forceinline__ float asf(unsigned u) { return __builtin_bit_cast(float, u); }
__device__ __forceinline__ unsigned asu(float f) { return __builtin_bit_cast(unsigned, f); }
__device__ __forceinline__ float blo(unsigned v) { return asf(v << 16); }
__device__ __forceinline__ float bhi(unsigned v) { return asf(v & 0xffff0000u); }
typedef __bf16 bf16x2_t __attribute__((ext_vector_type(2)));
__device__ __forceinline__ unsigned cvtpk(float lo, float hi) {
  return __builtin_bit_cast(unsigned, __builtin_convertvector(f32x2{lo, hi}, bf16x2_t));
}
__device__ __forceinline__ float rbf(float f) { return blo(cvtpk(f, f)); }
template <int CTRL> __device__ __forceinline__ float dpp(float x) {
  return asf(__builtin_amdgcn_mov_dpp((int)asu(x), CTRL, 0xf, 0xf, true));
}
constexpr int XOR1 = 0xB1, XOR2 = 0x4E, HMIR = 0x141, ROR8 = 0x128, ROR4 = 0x124;
__device__ __forceinline__ float xrow_sum(float x) {
  auto s = __builtin_amdgcn_permlane16_swap(asu(x), asu(x), false, false);
  x = asf(s[0]) + asf(s[1]);
  auto t = __builtin_amdgcn_permlane32_swap(asu(x), asu(x), false, false);
  return asf(t[0]) + asf(t[1]);
}
__device__ __forceinline__ float xrow_max(float x) {
  auto s = __builtin_amdgcn_permlane16_swap(asu(x), asu(x), false, false);
  x = fmaxf(asf(s[0]), asf(s[1]));
  auto t = __builtin_amdgcn_permlane32_swap(asu(x), asu(x), false, false);
  return fmaxf(asf(t[0]), asf(t[1]));
}
__device__ __forceinline__ float row_sum(float x) {
  x += dpp<XOR1>(x); x += dpp<XOR2>(x); x += dpp<HMIR>(x); x += dpp<ROR8>(x); return x;
}
__device__ __forceinline__ float row_max(float x) {
  x = fmaxf(x, dpp<XOR1>(x)); x = fmaxf(x, dpp<XOR2>(x)); x = fmaxf(x, dpp<HMIR>(x)); x = fmaxf(x, dpp<ROR8>(x)); return x;
}
__device__ __forceinline__ void fma8(float (&acc)[8], u32x4 w, float x) {
#pragma unroll
  for (int k = 0; k < 4; ++k) { acc[2 * k] = fmaf(x, blo(w[k]), acc[2 * k]); acc[2 * k + 1] = fmaf(x, bhi(w[k]), acc[2 * k + 1]); }
}
#define PUT_QUAD(r, voff, T, a, b, c, d, pol) PUT_PAIR(r, voff, T, asf(cvtpk(a, b)), asf(cvtpk(c, d)), pol)
#define PUT_PAIR(r, voff, T, a, b, pol) do { const float a_ = (a), b_ = (b); const int o_ = (voff); \
    const u32x4 v_ = {asu(a_), (T) ^ asu(b_), asu(b_), (T) ^ asu(a_)}; ST4Z(v_, r, o_, pol); } while (0)
__device__ __forceinline__ bool pair_ok(u32x4 v, unsigned T) { return ((v[1] ^ v[2]) == T) & ((v[3] ^ v[0]) == T); }
typedef unsigned long long u64;
#define BALLOT(c) __builtin_amdgcn_ballot_w64(c)
#define SETTLE1(r, o0, T, live, a, far_sleep, pol) \
  _Pragma("unroll 1") for (unsigned spins_ = 0; spins_ < SPIN_CAP; ++spins_) { \
    const u64 ma_ = BALLOT((live) & !pair_ok(a, T)); \
    if (ma_ == 0) break; \
    if (far_sleep && __popcll(ma_) > 16) __builtin_amdgcn_s_sleep(far_sleep); \
    u64 sv_; \
    asm volatile("s_mov_b64 %1, exec\n\ts_mov_b64 exec, %2\n\tbuffer_load_dwordx4 %0, %3, %4, 0 offen " pol "\n\ts_mov_b64 exec, %1\n\ts_waitcnt vmcnt(0)" \
                 : "+v"(a), "=&s"(sv_) : "s"(ma_), "v"(o0), "s"(r) : "memory"); }
#define RETRY_SLEEP 0
#define SETTLE2S(r, o0, o1, T, a, b, slp, pol) SETTLE2G(r, o0, o1, T, a, b, slp, -1, pol)
#define SETTLE2(r, o0, o1, T, a, b, pol) SETTLE2G(r, o0, o1, T, a, b, 0, 0, pol)
#define SETTLE2G(r, o0, o1, T, a, b, far_sleep, thr, pol) \
  _Pragma("unroll 1") for (unsigned spins_ = 0; spins_ < SPIN_CAP; ++spins_) { \
    const u64 ma_ = BALLOT(!pair_ok(a, T)), mb_ = BALLOT(!pair_ok(b, T)); \
    if ((ma_ | mb_) == 0) break; \
    if (far_sleep && (int)(__popcll(ma_) + __popcll(mb_)) > (thr)) __builtin_amdgcn_s_sleep(far_sleep); \
    u64 sv_; \
    asm volatile("s_mov_b64 %2, exec\n\ts_mov_b64 exec, %3\n\tbuffer_load_dwordx4 %0, %5, %7, 0 offen " pol "\n\t" \
                 "s_mov_b64 exec, %4\n\tbuffer_load_dwordx4 %1, %6, %7, 0 offen " pol "\n\ts_mov_b64 exec, %2\n\ts_waitcnt vmcnt(0)" \
                 : "+v"(a), "+v"(b), "=&s"(sv_) : "s"(ma_), "s"(mb_), "v"(o0), "v"(o1), "s"(r) : "memory"); }
#define SETTLE4(r, o0, o1, o2, o3, T, a, b, c, d, pol) \
  _Pragma("unroll 1") for (unsigned spins_ = 0; spins_ < SPIN_CAP; ++spins_) { \
    const u64 m0_ = BALLOT(!(pair_ok(a, T) & pair_ok(b, T))), m1_ = BALLOT(!(pair_ok(c, T) & pair_ok(d, T))); \
    if ((m0_ | m1_) == 0) break; \
    u64 sv_; \
    asm volatile("s_mov_b64 %4, exec\n\ts_mov_b64 exec, %5\n\tbuffer_load_dwordx4 %0, %7, %11, 0 offen " pol "\n\tbuffer_load_dwordx4 %1, %8, %11, 0 offen " pol "\n\t" \
                 "s_mov_b64 exec, %6\n\tbuffer_load_dwordx4 %2, %9, %11, 0 offen " pol "\n\tbuffer_load_dwordx4 %3, %10, %11, 0 offen " pol "\n\t" \
                 "s_mov_b64 exec, %4\n\ts_waitcnt vmcnt(0)" \
                 : "+v"(a), "+v"(b), "+v"(c), "+v"(d), "=&s"(sv_) : "s"(m0_), "s"(m1_), "v"(o0), "v"(o1), "v"(o2), "v"(o3), "s"(r) : "memory"); }
__device__ __forceinline__ f32x4 rowmix(unsigned u2, u32x4 msk, u32x4 wreg, f32x4 acc) {
  const u32x4 a = {u2 & msk[0], u2 & msk[1], u2 & msk[2], u2 & msk[3]};
  return __builtin_amdgcn_mfma_f32_16x16x32_bf16(__builtin_bit_cast(s16x8, a), __builtin_bit_cast(s16x8, wreg), acc, 0, 0, 0);
}
__device__ __forceinline__ float fold32(float a, float b) {
  auto s = __builtin_amdgcn_permlane32_swap(asu(a), asu(b), false, false);
  return asf(s[0]) + asf(s[1]);
}
__device__ __forceinline__ float fold16(float a, float b) {
  auto s = __builtin_amdgcn_permlane16_swap(asu(a), asu(b), false, false);
  return asf(s[0]) + asf(s[1]);
}
struct Lds {
  float qred[8][64];
  float qh[2][256];
  float hb[6][292];
  float ob[4][256];
  float ml[4][2][2];
  unsigned um[4][12];
  unsigned ux[8][6];
  float fin[4][4];
  float gt[2];
};

__global__ void __launch_bounds__(256, 2)
fused_decode(const u16* __restrict__ x, const u16* __restrict__ kc, const u16* __restrict__ vc, const u16* __restrict__ wq,
             const u16* __restrict__ wo, const u16* __restrict__ w1, const u16* __restrict__ b1, const u16* __restrict__ w2,
             const u16* __restrict__ b2, const u16* __restrict__ gw, unsigned char* ws, u16* out) {
  __shared__ __attribute__((aligned(16))) Lds L;
  unsigned long long ts[STAMP_PER_WG];
  for (int i = 0; i < STAMP_PER_WG; ++i) ts[i] = 0;
  STAMP(0);
  unsigned c0 = 0;
  if (PACE) { unsigned long long n_; asm volatile("s_memtime %0\n\ts_waitcnt lgkmcnt(0)" : "=s"(n_)); c0 = (unsigned)n_; }
  const int b = blockIdx.x, t = threadIdx.x, l = t & 63, w = __builtin_amdgcn_readfirstlane(t >> 6);
  const int h = ((b & 7) << 1) | ((b >> 3) & 1), s = b >> 4, gid = h * 16 + s;
#define XS 4
  const int hc = (h + 2 * XS) & 15, gidc = hc * 16 + s;
  const int cb = s & 3, rg = s >> 2;

  const srd_t rws = mk(ws, WS_END);
  const srd_t rx = mk(x, DM * 2), rgw = mk(gw, DM * 2);
#define XQ 4
  const int hq = (h + 2 * XQ) & 15;
  const srd_t rwq = mk(wq + (size_t)(144 * rg) * NQ + hq * 256 + cb * 64, 143 * 8192 + 128);
  const size_t kvo = ((size_t)h * SK + (SK - WIN) + s * 256) * DH;
  const srd_t rk = mk(kc + kvo, 65536), rv = mk(vc + kvo, 65536);
  const srd_t rw1 = mk(w1 + (size_t)(36 * s) * FF + h * 288, 35 * 9216 + 576);
  const srd_t rw2 = mk(w2 + (size_t)gidc * 18 * DM, 18 * 1152);
  const srd_t rwo = mk(wo + (size_t)gidc * 16 * DM, 16 * 1152);
  const srd_t rb1 = mk(b1 + gidc * 18, 36);

  const srd_t rb2 = mk(b2, DM * 2), rout = mk(out, DM * 2);
  unsigned epoch, xq4, ptq; u32x2 xq; u32x4 aq[5];
  {
    const int g = l >> 3, xo = (144 * rg + 36 * w) * 2;
    const int o0 = xo + 8 * g, o1 = g < 4 ? xo + 64 + 2 * g : OOB, z = WS_EPOCH;
    const int vo = (36 * w + 4 * g) * 8192 + (l & 7) * 16, v4 = g < 4 ? (36 * w + 32 + g) * 8192 + (l & 7) * 16 : OOB;
#pragma unroll
    for (int j = 0; j < 4; ++j) LD4(aq[j], rwq, vo, j * 8192, "nt");
    LD4Z(aq[4], rwq, v4, "nt");
    LD2Z(xq, rx, o0, ""); LDHZ(xq4, rx, o1, "");
    LD1Z(epoch, rws, z, "sc1");
    const int pq = PRETOUCH_Q && t < 4 ? WS_Q + (hq * 16 + s) * 512 + t * 128 : OOB;
    LD1Z(ptq, rws, pq, "");
  }
  u32x4 ak[16], av[16];
  const int kvoff = w * 1024 + ((l & 15) >> 2) * 256 + (l & 3) * 16 + (l >> 4) * 64;
  const int vvoff = w * 1024 + l * 16;
  if (STAGGER) __builtin_amdgcn_s_sleep(STAGGER);
#pragma unroll
  for (int j = 0; j < K0; ++j) { if (j + 5 > PACE0) PACED(j + 5); LD4(ak[j], rk, kvoff, j * 4096, "nt"); }
  u32x4 msk;
  {
    const unsigned half = (l & 1) ? 0xffff0000u : 0x0000ffffu;
#pragma unroll
    for (int k = 0; k < 4; ++k) { msk[k] = (((l & 7) >> 1) == k) ? half : 0u; TIE(msk[k]); }
  }
  if (t >= 128 && t < 128 + 96) (&L.um[0][0])[t - 128] = 0u;
  int qpo = t < 32 ? WS_Q + (hq * 16 + s) * 512 + t * 16 : OOB;
  TIE(qpo);
  PIN();
  STAMP(12);
  WAITN(K0); TIE(ptq); TIE(epoch); TIE(xq); TIE(xq4); TIE(aq[0]); TIE(aq[1]); TIE(aq[2]); TIE(aq[3]); TIE(aq[4]);
  STAMP(1);
  unsigned T = __builtin_amdgcn_readfirstlane(epoch) + 1u;
  T = T ? T : 1u;
  {
    f32x4 acc = {0.f, 0.f, 0.f, 0.f};
    acc = rowmix(__builtin_amdgcn_perm(xq[0], xq[0], 0x01000100u), msk, aq[0], acc);
    acc = rowmix(__builtin_amdgcn_perm(xq[0], xq[0], 0x03020302u), msk, aq[1], acc);
    acc = rowmix(__builtin_amdgcn_perm(xq[1], xq[1], 0x01000100u), msk, aq[2], acc);
    acc = rowmix(__builtin_amdgcn_perm(xq[1], xq[1], 0x03020302u), msk, aq[3], acc);
    acc = rowmix(__builtin_amdgcn_perm(xq4, xq4, 0x01000100u), msk, aq[4], acc);
    if ((l >> 5) == ((l >> 3) & 1)) *(f32x4*)&L.qred[2 * w + (l >> 5)][8 * (l & 7) + 4 * ((l >> 4) & 1)] = acc;
  }
  __syncthreads();
  {
    f32x2 a = *(const f32x2*)&L.qred[0][2 * (t & 31)];
#pragma unroll
    for (int i = 1; i < 8; ++i) a += *(const f32x2*)&L.qred[i][2 * (t & 31)];
    PUT_PAIR(rws, qpo, T, a[0], a[1], POL_Q_ST);
  }
  PIN();
  STAMP(2);
#pragma unroll
  for (int j = K0; j < 16; ++j) { PACED(j + 5); LD4(ak[j], rk, kvoff, j * 4096, "nt"); }
  u32x4 a1[6]; u32x3 x1; unsigned b1v, pt;
  const int r6 = t / 36, c36 = t - r6 * 36;
  {
    const int vo = t < 216 ? r6 * 6 * 9216 + c36 * 16 : OOB, xo = (36 * s + 6 * min(r6, 5)) * 2;
    const int bo = t >= 64 ? (((t - 64) >> 4) * 4 + (t & 3)) * 2 : OOB;
    const int po = PRETOUCH && t < 9 ? WS_O + gid * O_ROW + t * 128 : OOB;
    LD3Z(x1, rx, xo, ""); LDHZ(b1v, rb1, bo, ""); LD1Z(pt, rws, po, "");
#pragma unroll
    for (int j = 0; j < 6; ++j) { PACED(21 + (j * 5) / 6); LD4(a1[j], rw1, vo, j * 9216, "nt"); }
  }
  const int qo0 = WS_Q + (h * 16 + (t >> 7) * 8 + ((t & 127) >> 5)) * 512 + (t & 31) * 16, qo1 = qo0 + 4 * 512;
  u32x4 pa, pb;
#pragma unroll
  for (int j = 0; j < QP; ++j) LD4(av[j], rv, vvoff, j * 4096, "nt");
  LD4Z(pa, rws, qo0, POL_Q_LD); LD4Z(pb, rws, qo1, POL_Q_LD);
#pragma unroll
  for (int j = QP; j < VPRE; ++j) { PACED(26 + j); LD4(av[j], rv, vvoff, j * 4096, "nt"); }
  PIN();
  STAMP(3);
  {
    float acc[8] = {0.f, 0.f, 0.f, 0.f, 0.f, 0.f, 0.f, 0.f};
    WAITN(VPRE + 2); TIE(x1); TIE(b1v); TIE(pt);
#pragma unroll
    for (int j = 0; j < 6; ++j) TIE(a1[j]);
#pragma unroll
    for (int j = 0; j < 16; ++j) TIE(ak[j]);
#pragma unroll
    for (int j = 0; j < 6; ++j) fma8(acc, a1[j], (j & 1) ? bhi(x1[j >> 1]) : blo(x1[j >> 1]));
    if (t < 216) {
      *(f32x4*)&L.hb[r6][8 * c36] = f32x4{acc[0], acc[1], acc[2], acc[3]};
      *(f32x4*)&L.hb[r6][8 * c36 + 4] = f32x4{acc[4], acc[5], acc[6], acc[7]};
    }
  }
  PIN();
  STAMP(15);
  WAITN(VPRE - QP); TIE(pa); TIE(pb);
  SETTLE2(rws, qo0, qo1, T, pa, pb, POL_Q_LD);
  *(f32x2*)&L.qh[t >> 7][2 * (t & 127)] = f32x2{asf(pa[0]) + asf(pb[0]), asf(pa[2]) + asf(pb[2])};
  __syncthreads();
  STAMP(4);
  u32x4 qf;
  {
    const int o = ((l >> 3) & 1) * 128 + ((l & 3) + 4 * (l >> 4)) * 8;
    const f32x4 a0 = *(const f32x4*)&L.qh[0][o] + *(const f32x4*)&L.qh[1][o];
    const f32x4 a1_ = *(const f32x4*)&L.qh[0][o + 4] + *(const f32x4*)&L.qh[1][o + 4];
    qf = u32x4{cvtpk(a0[0], a0[1]), cvtpk(a0[2], a0[3]), cvtpk(a1_[0], a1_[1]), cvtpk(a1_[2], a1_[3])};
  }
  constexpr float C2 = 0.08838834764831845f * 1.4426950408889634f;
  u32x4 a2[5], ao[4], a2x[3], aox[2];
  const int rho = l >> 4;
  const int wvo = rho * 1152 + w * 256 + (l & 15) * 16;
  const int x2o = w == 3 ? (l >> 3) * 1152 + 1024 + (l & 7) * 16 : OOB;
  float sc[16];
  {
    f32x4 d[16];
#pragma unroll
    for (int j = 0; j < 16; ++j)
      d[j] = __builtin_amdgcn_mfma_f32_16x16x32_bf16(__builtin_bit_cast(s16x8, ak[j]), __builtin_bit_cast(s16x8, qf), f32x4{0.f, 0.f, 0.f, 0.f}, 0, 0, 0);
    PIN();
#pragma unroll
    for (int j = VPRE; j < (VPRE + 16) / 2; ++j) LD4(av[j], rv, vvoff, j * 4096, "nt");
    PIN();
#pragma unroll
    for (int j = 0; j < 16; ++j) sc[j] = (dpp<0x00>(d[j][0]) + dpp<0x55>(d[j][1])) + (dpp<0xAA>(d[j][2]) + dpp<0xFF>(d[j][3]));
  }
  PIN();
#pragma unroll
  for (int j = (VPRE + 16) / 2; j < 16; ++j) LD4(av[j], rv, vvoff, j * 4096, "nt");
  PIN();
  float mx = -INFINITY;
#pragma unroll
  for (int j = 0; j < 16; ++j) mx = fmaxf(mx, sc[j]);
  mx = xrow_max(mx) * C2;
  float lsum = 0.f;
  unsigned pk[16];
#pragma unroll
  for (int j = 0; j < 16; ++j) { const float p = __builtin_amdgcn_exp2f(fmaf(sc[j], C2, -mx)); lsum += p; pk[j] = cvtpk(p, p); }
  lsum = xrow_sum(lsum);
  PIN();
  STAMP(5);
  f32x4 oacc = {0.f, 0.f, 0.f, 0.f};
  {
    const int tt = min(t, 79), j = tt / 5, c0 = 18 * j + 4 * (tt - 5 * j);
    f32x2 a = *(const f32x2*)&L.hb[0][c0], c = *(const f32x2*)&L.hb[0][c0 + 2];
#pragma unroll
    for (int i = 1; i < 6; ++i) { a += *(const f32x2*)&L.hb[i][c0]; c += *(const f32x2*)&L.hb[i][c0 + 2]; }
    PUT_QUAD(rws, t < 80 ? WS_H + gid * H_ROW + t * 16 : OOB, T, a[0], a[1], c[0], c[1], POL_H_ST);
  }
  PIN();
  u32x4 xa, xb, ga, gb; unsigned b2v;
  const int go0 = w == 3 ? l * 16 : OOB, go1 = w == 3 ? l * 16 + 1024 : OOB;
  const int fin = (b & 7) * 18 + (b >> 3);
  const int ocol = (fin < 128 ? (fin >> 5) * 128 + (fin & 15) * 8 + ((fin >> 4) & 1) * 4 : 512 + (fin & 7) * 8 + ((fin >> 3) & 1) * 4) + (t & 3);
  const int b2o = (b >> 3) < 18 ? ocol * 2 : OOB;
  int opo = WS_O + gid * O_ROW + l * 16, mpo = (l & 31) == 0 ? WS_O + gid * O_ROW + 1024 + (l >> 5) * 16 : OOB;
  if (ABL & 1) { opo = OOB; mpo = OOB; }
  TIE(opo); TIE(mpo);
#define PV_STEP(j, n) { WAITV1(n, av[j]); oacc = rowmix(pk[j], msk, av[j], oacc); PIN(); }
  PV_STEP(0, 16)  LD4(a2[0], rw2, wvo, 0, "nt");
  PV_STEP(1, 16)  LD4(a2[1], rw2, wvo, 4608, "nt");
  PV_STEP(2, 16)  LD4(a2[2], rw2, wvo, 2 * 4608, "nt");
  PV_STEP(3, 16)  LD4(a2[3], rw2, wvo, 3 * 4608, "nt");
  PV_STEP(4, 16)  LD4(a2[4], rw2, wvo, 4 * 4608, "nt");
  PV_STEP(5, 16)  LD4(a2x[0], rw2, x2o, 0, "nt");
  PV_STEP(6, 16)  LD4(a2x[1], rw2, x2o, 9216, "nt");
  PV_STEP(7, 16)  LD4(a2x[2], rw2, x2o, 2 * 9216, "nt");
  PV_STEP(8, 16)  LD4(ao[0], rwo, wvo, 0, "nt");
  PV_STEP(9, 16)  LD4(ao[1], rwo, wvo, 4608, "nt");
  PV_STEP(10, 16) LD4(ao[2], rwo, wvo, 2 * 4608, "nt");
  PV_STEP(11, 16) LD4(ao[3], rwo, wvo, 3 * 4608, "nt");
  PV_STEP(12, 16) LD4(aox[0], rwo, x2o, 0, "nt");
  PV_STEP(13, 16) LD4(aox[1], rwo, x2o, 9216, "nt");
  PV_STEP(14, 16) LD4Z(xa, rx, go0, ""); LD4Z(xb, rx, go1, ""); LD4Z(ga, rgw, go0, ""); LD4Z(gb, rgw, go1, ""); LDHZ(b2v, rb2, b2o, "");
  PV_STEP(15, 20)
  *(f32x4*)&L.ob[w][(l >> 5) * 128 + (l & 15) * 8 + ((l >> 4) & 1) * 4] = oacc;
  if ((l & 55) == 0) *(f32x2*)&L.ml[w][l >> 3][0] = f32x2{mx, lsum};
  STAMP(6);
  __syncthreads();
  if (w == 3) {
    const int hh = l >> 5, oi = 4 * l;
    const f32x2 q0 = *(const f32x2*)&L.ml[0][hh][0], q1 = *(const f32x2*)&L.ml[1][hh][0], q2 = *(const f32x2*)&L.ml[2][hh][0], q3 = *(const f32x2*)&L.ml[3][hh][0];
    const f32x4 o0 = *(const f32x4*)&L.ob[0][oi], o1 = *(const f32x4*)&L.ob[1][oi], o2 = *(const f32x4*)&L.ob[2][oi], o3 = *(const f32x4*)&L.ob[3][oi];
    const float M = fmaxf(fmaxf(q0[0], q1[0]), fmaxf(q2[0], q3[0]));
    const float e0 = __builtin_amdgcn_exp2f(q0[0] - M), e1 = __builtin_amdgcn_exp2f(q1[0] - M), e2 = __builtin_amdgcn_exp2f(q2[0] - M), e3 = __builtin_amdgcn_exp2f(q3[0] - M);
    const f32x4 v = e0 * o0 + e1 * o1 + e2 * o2 + e3 * o3;
    PUT_QUAD(rws, opo, T, v[0], v[1], v[2], v[3], POL_O_ST);
    PUT_PAIR(rws, mpo, T, M, e0 * q0[1] + e1 * q1[1] + e2 * q2[1] + e3 * q3[1], POL_O_ST);
  }
  PIN();
  STAMP(7);
  float ngate;
  {
    const int th_ = t - 64;
    const int ob_ = (ABL & 2) ? OOB : WS_O + (hc * 16 + (l >> 2)) * O_ROW;
    const bool hl = th_ >= 0 && th_ < 80;
    const int oo = w == 0 ? ob_ + s * 64 + (l & 3) * 16 : hl ? WS_H + (hc * 16 + (t & 15)) * H_ROW + (s * 5 + (th_ >> 4)) * 16 : OOB;
    const int om = w == 0 ? ob_ + 1024 + (s >> 3) * 16 : OOB;
    const int row = w == 0 ? 4 * (l & 3) + rho : 4 * (th_ >> 4) + (l & 3);
    int slot = (l & 4) ? 48 + (row & 7) * 6 + (row >> 3) + (w == 0 ? 0 : 2) : (row & 3) * 12 + (row >> 2) + (w == 0 ? 0 : 4);
    const bool wr = (l & 8) == 0 && (w == 0 || (hl && row < 18));
    int yo = l < 32 ? WS_Y + b * Y_ROW + (32 * w + l) * 16 : OOB;
    int yxo = (l & 40) == 0 ? WS_Y + b * Y_ROW + (128 + 8 * rho + (l & 7)) * 16 : OOB;
    TIE(slot); TIE(yo); TIE(yxo);
    u32x4 p0, m0;
    if (w < 3) {
      WAITN(11);
      if (O_SLEEP) __builtin_amdgcn_s_sleep(O_SLEEP);
      STAMP(13);
      LD4Z(p0, rws, oo, POL_O_LD); LD4Z(m0, rws, om, POL_O_LD);
      WAITN(2);
      STAMP(14);
      WAITV2(0, p0, m0);
    } else {
      WAITN(2);
      p0 = m0 = u32x4{0u, 0u, 0u, 0u};
    }
    TIE(b2v); TIE(xa); TIE(xb); TIE(ga); TIE(gb);
#pragma unroll
    for (int j = 0; j < 5; ++j) TIE(a2[j]);
#pragma unroll
    for (int j = 0; j < 4; ++j) TIE(ao[j]);
    TIE(a2x[0]); TIE(a2x[1]); TIE(a2x[2]); TIE(aox[0]); TIE(aox[1]);
    float v = 0.f;
    if (w == 0) {
      if (!(ABL & 4)) SETTLE2S(rws, oo, om, T, p0, m0, RETRY_SLEEP, POL_O_LD);
      STAMP(8);
      float M = asf(m0[0]);
      M = fmaxf(M, dpp<ROR4>(M)); M = fmaxf(M, dpp<ROR8>(M)); M = xrow_max(M);
      const float e = __builtin_amdgcn_exp2f(asf(m0[0]) - M);
      float Ls = e * asf(m0[2]);
      v = fold16(fold32(e * blo(p0[0]), e * blo(p0[2])), fold32(e * bhi(p0[0]), e * bhi(p0[2])));
      Ls += dpp<ROR4>(Ls); v += dpp<ROR4>(v);
      Ls += dpp<ROR8>(Ls); v += dpp<ROR8>(v);
      Ls = xrow_sum(Ls);
      v *= __builtin_amdgcn_rcpf(Ls);
    } else if (w < 3) {
      SETTLE1(rws, oo, T, hl, p0, 0, POL_O_LD);
      const float s0 = row_sum(blo(p0[0])), s1 = row_sum(bhi(p0[0])), s2 = row_sum(blo(p0[2])), s3 = row_sum(bhi(p0[2]));
      const float sv = (t & 2) ? ((t & 1) ? s3 : s2) : ((t & 1) ? s1 : s0);
      const float hv = rbf(rbf(sv) + blo(b1v));
      const float z = 0.7978845608028654f * (hv + 0.044715f * hv * hv * hv);
      const float th = 1.f - 2.f * __builtin_amdgcn_rcpf(1.f + __expf(2.f * z));
      v = 0.5f * hv * (1.f + th);
    } else {
      float g = 0.f;
#pragma unroll
      for (int k = 0; k < 4; ++k) {
        const unsigned q0 = cvtpk(blo(xa[k]) * blo(ga[k]), bhi(xa[k]) * bhi(ga[k])), q1 = cvtpk(blo(xb[k]) * blo(gb[k]), bhi(xb[k]) * bhi(gb[k]));
        g += (blo(q0) + bhi(q0)) + (blo(q1) + bhi(q1));
      }
      g = rbf(xrow_sum(row_sum(g)));
      g = rbf(__builtin_amdgcn_rcpf(1.f + __expf(-g)));
      if (l == 0) *(f32x2*)L.gt = f32x2{g, rbf(1.f - g)};
    }
    if (wr) (&L.um[0][0])[slot] = cvtpk(v, v);
    __syncthreads();
    const u32x4 uo = *(const u32x4*)&L.um[rho][0], ua = *(const u32x4*)&L.um[rho][4];
    const unsigned ua4 = L.um[rho][8];
    const f32x2 gt = *(const f32x2*)L.gt;
    const float gate = gt[0];
    ngate = gt[1];
    f32x4 ya = {0.f, 0.f, 0.f, 0.f}, yb = {0.f, 0.f, 0.f, 0.f};
#pragma unroll
    for (int j = 0; j < 4; ++j) { ya = rowmix(uo[j], msk, ao[j], ya); yb = rowmix(ua[j], msk, a2[j], yb); }
    yb = rowmix(ua4, msk, a2[4], yb);
    const f32x4 y = gate * ya + ngate * yb;
    PUT_QUAD(rws, yo, T, y[0], y[1], y[2], y[3], POL_Y_ST);
    if (w == 3) {
      const unsigned* ux = &L.ux[l >> 3][0];
      f32x4 xa = {0.f, 0.f, 0.f, 0.f}, xb = {0.f, 0.f, 0.f, 0.f};
      xa = rowmix(ux[0], msk, aox[0], xa); xb = rowmix(ux[2], msk, a2x[0], xb);
      xa = rowmix(ux[1], msk, aox[1], xa); xb = rowmix(ux[3], msk, a2x[1], xb);
      xb = rowmix(ux[4], msk, a2x[2], xb);
      f32x4 z = gate * xa + ngate * xb;
#pragma unroll
      for (int k = 0; k < 4; ++k) {
        const float r = asf(__builtin_amdgcn_update_dpp((int)asu(z[k]), (int)asu(z[k]), ROR8, 0xC, 0xF, false));
        z[k] = fold32(r, r);
      }
      PUT_QUAD(rws, yxo, T, z[0], z[1], z[2], z[3], POL_Y_ST);
    }
  }
  PIN();
  STAMP(9);
  if ((b >> 3) < 18) {
    const int o0 = WS_Y + t * Y_ROW + fin * 16;
    __builtin_amdgcn_s_sleep(FIN_SLEEP);
    LD4Z(pa, rws, o0, POL_Y_LD);
    WAITV1(0, pa);
    SETTLE1(rws, o0, T, true, pa, FAR_SLEEP_3, POL_Y_LD);
    STAMP(10);
    const float v = row_sum(fold16(fold32(blo(pa[0]), blo(pa[2])), fold32(bhi(pa[0]), bhi(pa[2]))));
    if ((l & 15) == 0) L.fin[w][rho] = v;
    __syncthreads();
    if (t < 4) {
      const float r = ((L.fin[0][t] + L.fin[1][t]) + (L.fin[2][t] + L.fin[3][t])) + ngate * blo(b2v);
      const unsigned pr = cvtpk(r, r); const int o = ocol * 2;
      STHZ(pr, rout, o, "");
    }
    if (b == 0 && t == 0) { const int z = WS_EPOCH; const unsigned tv = T; ST1Z(tv, rws, z, "sc1"); }
  }
  STAMP(11);
  if (t == 0) { unsigned long long* sp = (unsigned long long*)(ws + STAMP_OFF) + b * STAMP_PER_WG; for (int i = 0; i < STAMP_PER_WG; ++i) sp[i] = ts[i] ? ts[i] : ts[9]; }
}

static void kernel_launch_inner(void* const* d_in, const int* in_sizes, int n_in, void* d_out, int out_size, void* d_ws, size_t ws_size, hipStream_t stream) {
  fused_decode<<<256, 256, 0, stream>>>((const u16*)d_in[0], (const u16*)d_in[1], (const u16*)d_in[2], (const u16*)d_in[3], (const u16*)d_in[4],
                                        (const u16*)d_in[6], (const u16*)d_in[7], (const u16*)d_in[8], (const u16*)d_in[9], (const u16*)d_in[10],
                                        (unsigned char*)d_ws, (u16*)d_out);
}

__global__ void spin_report(const unsigned char* ws) {
  const unsigned long long* sp = (const unsigned long long*)(ws + STAMP_OFF);
  const int w = threadIdx.x;
  __shared__ long long a0[256], ak[256];
  a0[w] = sp[w * STAMP_PER_WG]; ak[w] = sp[w * STAMP_PER_WG + 3];
  __syncthreads();
  if (w == 0) {
    long long t0 = a0[0];
    for (int i = 0; i < 256; ++i) t0 = a0[i] < t0 ? a0[i] : t0;
    long long lo = 0, hi = 0, mlo = 0, mhi = 0;
    for (int i = 0; i < 256; ++i) { const long long v = ak[i] - t0; if ((i & 7) < 4) { lo += v; mlo = v > mlo ? v : mlo; } else { hi += v; mhi = v > mhi ? v : mhi; } }
    lo /= 128; hi /= 128;
    long long m = 200 + (mhi - mlo);
    if (m > 5000) m = 5000; if (m < 0) m = 0;
    unsigned long long st = __builtin_amdgcn_s_memrealtime();
    while (__builtin_amdgcn_s_memrealtime() - st < 100 * (unsigned long long)m) __builtin_amdgcn_s_sleep(8);
  }
}
extern "C" void kernel_launch(void* const* d_in, const int* in_sizes, int n_in, void* d_out, int out_size, void* d_ws, size_t ws_size, hipStream_t stream) {
  kernel_launch_inner(d_in, in_sizes, n_in, d_out, out_size, d_ws, ws_size, stream);
  spin_report<<<1, 256, 0, stream>>>((const unsigned char*)d_ws);
}
```

```cpp
#include <hip/hip_runtime.h>
#include <hip/hip_bf16.h>
#include <stdint.h>

typedef unsigned short u16;
typedef unsigned u32x4 __attribute__((ext_vector_type(4)));
typedef unsigned u32x3 __attribute__((ext_vector_type(3)));
typedef unsigned u32x2 __attribute__((ext_vector_type(2)));
typedef unsigned u32x8 __attribute__((ext_vector_type(8)));
typedef float f32x4 __attribute__((ext_vector_type(4)));
typedef float f32x2 __attribute__((ext_vector_type(2)));
typedef short s16x8 __attribute__((ext_vector_type(8)));

constexpr int DM = 576, HKV = 16, SK = 32768, DH = 128, WIN = 4096, FF = 4608, NQ = 4096;
constexpr int OOB = 0x40000000;

constexpr unsigned WS_EPOCH = 0;
constexpr unsigned WS_Q = 4096;
constexpr unsigned H_ROW = 1280, O_ROW = 1152, Y_ROW = 2304;
constexpr unsigned WS_H = WS_Q + 256 * 512;
constexpr unsigned WS_O = WS_H + 256 * H_ROW;
constexpr unsigned WS_Y = WS_O + 256 * O_ROW;
constexpr unsigned WS_END = WS_Y + 256 * Y_ROW;
constexpr unsigned SPIN_CAP = 1u << 18;
#define FAR_SLEEP_3 4
#define POL_Q_ST "sc1 nt"
#define POL_Q_LD "sc1 nt"
#define POL_H_ST "sc1 nt"
#define POL_O_ST "sc1"
#define POL_ML_ST "sc1"
#define POL_H_LD "sc1 nt"
#define K1 12
#define POL_O_LD "sc1"
#define POL_Y_ST "sc1"
#define POL_Y_LD "sc1"
#define PRETOUCH 0
#define STAGGER 0
#define QP 0
#define VPRE 12
#define CODE_WARM 0
#define PRETOUCH_Q 0
#define ABL 0
#define PACE 0
#define PACE0 8
#define PACED(step) do { if (PACE) { const unsigned tgt_ = c0 + (unsigned)(((step) - PACE0) * PACE); unsigned long long n_; \
    do { asm volatile("s_memtime %0\n\ts_waitcnt lgkmcnt(0)" : "=s"(n_)); } while ((int)((unsigned)n_ - tgt_) < 0); } } while (0)
#define GAP 5
#define FAR_N 16
#define QB 6
#define N1 2
#define N2 2
#define N3 1
#define N4 1
#define N5 2
#define HP 12
#define K0 7
#define O_SLEEP 8
#define FIN_SLEEP 16

#define STAMP(k) do {} while (0)
#define PIN() __builtin_amdgcn_sched_barrier(0)

typedef u32x4 srd_t;
__device__ __forceinline__ srd_t mk(const void* p, unsigned bytes) {
  const unsigned long long a = (unsigned long long)p;
  return srd_t{(unsigned)a, (unsigned)(a >> 32), bytes, 0x00020000u};
}
#define LD4(dst, srd, voff, soff, pol) asm volatile("buffer_load_dwordx4 %0, %1, %2, %3 offen " pol : "=v"(dst) : "v"(voff), "s"(srd), "s"(soff))
#define LD4Z(dst, srd, voff, pol) asm volatile("buffer_load_dwordx4 %0, %1, %2, 0 offen " pol : "=v"(dst) : "v"(voff), "s"(srd))
#define LD3Z(dst, srd, voff, pol) asm volatile("buffer_load_dwordx3 %0, %1, %2, 0 offen " pol : "=v"(dst) : "v"(voff), "s"(srd))
#define LD2Z(dst, srd, voff, pol) asm volatile("buffer_load_dwordx2 %0, %1, %2, 0 offen " pol : "=v"(dst) : "v"(voff), "s"(srd))
#define LD1Z(dst, srd, voff, pol) asm volatile("buffer_load_dword %0, %1, %2, 0 offen " pol : "=v"(dst) : "v"(voff), "s"(srd))
#define LDHZ(dst, srd, voff, pol) asm volatile("buffer_load_ushort %0, %1, %2, 0 offen " pol : "=v"(dst) : "v"(voff), "s"(srd))
#define ST4Z(val, srd, voff, pol) asm volatile("buffer_store_dwordx4 %0, %1, %2, 0 offen " pol "\n\ts_nop 1" :: "v"(val), "v"(voff), "s"(srd) : "memory")
#define ST1Z(val, srd, voff, pol) asm volatile("buffer_store_dword %0, %1, %2, 0 offen " pol :: "v"(val), "v"(voff), "s"(srd) : "memory")
#define STHZ(val, srd, voff, pol) asm volatile("buffer_store_short %0, %1, %2, 0 offen " pol :: "v"(val), "v"(voff), "s"(srd) : "memory")
#define WAITV1(n, a) asm volatile("s_waitcnt vmcnt(" #n ")" : "+v"(a))
#define WAITV2(n, a, b) asm volatile("s_waitcnt vmcnt(" #n ")" : "+v"(a), "+v"(b))
#define WAITV3(n, a, b, c) asm volatile("s_waitcnt vmcnt(" #n ")" : "+v"(a), "+v"(b), "+v"(c))
#define WAITN(n) asm volatile("s_waitcnt vmcnt(%0)" :: "i"(n))
#define TIE(a) asm volatile("" : "+v"(a))
__device__ __forceinline__ float asf(unsigned u) { return __builtin_bit_cast(float, u); }
__device__ __forceinline__ unsigned asu(float f) { return __builtin_bit_cast(unsigned, f); }
__device__ __forceinline__ float blo(unsigned v) { return asf(v << 16); }
__device__ __forceinline__ float bhi(unsigned v) { return asf(v & 0xffff0000u); }
typedef __bf16 bf16x2_t __attribute__((ext_vector_type(2)));
__device__ __forceinline__ unsigned cvtpk(float lo, float hi) {
  return __builtin_bit_cast(unsigned, __builtin_convertvector(f32x2{lo, hi}, bf16x2_t));
}
__device__ __forceinline__ float rbf(float f) { return blo(cvtpk(f, f)); }
template <int CTRL> __device__ __forceinline__ float dpp(float x) {
  return asf(__builtin_amdgcn_mov_dpp((int)asu(x), CTRL, 0xf, 0xf, true));
}
constexpr int XOR1 = 0xB1, XOR2 = 0x4E, HMIR = 0x141, ROR8 = 0x128, ROR4 = 0x124;
__device__ __forceinline__ float xrow_sum(float x) {
  auto s = __builtin_amdgcn_permlane16_swap(asu(x), asu(x), false, false);
  x = asf(s[0]) + asf(s[1]);
  auto t = __builtin_amdgcn_permlane32_swap(asu(x), asu(x), false, false);
  return asf(t[0]) + asf(t[1]);
}
__device__ __forceinline__ float xrow_max(float x) {
  auto s = __builtin_amdgcn_permlane16_swap(asu(x), asu(x), false, false);
  x = fmaxf(asf(s[0]), asf(s[1]));
  auto t = __builtin_amdgcn_permlane32_swap(asu(x), asu(x), false, false);
  return fmaxf(asf(t[0]), asf(t[1]));
}
__device__ __forceinline__ float row_sum(float x) {
  x += dpp<XOR1>(x); x += dpp<XOR2>(x); x += dpp<HMIR>(x); x += dpp<ROR8>(x); return x;
}
__device__ __forceinline__ float row_max(float x) {
  x = fmaxf(x, dpp<XOR1>(x)); x = fmaxf(x, dpp<XOR2>(x)); x = fmaxf(x, dpp<HMIR>(x)); x = fmaxf(x, dpp<ROR8>(x)); return x;
}
__device__ __forceinline__ void fma8(float (&acc)[8], u32x4 w, float x) {
#pragma unroll
  for (int k = 0; k < 4; ++k) { acc[2 * k] = fmaf(x, blo(w[k]), acc[2 * k]); acc[2 * k + 1] = fmaf(x, bhi(w[k]), acc[2 * k + 1]); }
}
#define PUT_QUAD(r, voff, T, a, b, c, d, pol) PUT_PAIR(r, voff, T, asf(cvtpk(a, b)), asf(cvtpk(c, d)), pol)
#define PUT_PAIR(r, voff, T, a, b, pol) do { const float a_ = (a), b_ = (b); const int o_ = (voff); \
    const u32x4 v_ = {asu(a_), (T) ^ asu(b_), asu(b_), (T) ^ asu(a_)}; ST4Z(v_, r, o_, pol); } while (0)
__device__ __forceinline__ bool pair_ok(u32x4 v, unsigned T) { return ((v[1] ^ v[2]) == T) & ((v[3] ^ v[0]) == T); }
typedef unsigned long long u64;
#define BALLOT(c) __builtin_amdgcn_ballot_w64(c)
#define SETTLE1(r, o0, T, live, a, far_sleep, pol) { \
  u64 ma_ = BALLOT((live) & !pair_ok(a, T)); \
  if (__builtin_expect(ma_ != 0, 0)) { \
    _Pragma("unroll 1") for (unsigned spins_ = 0; spins_ < SPIN_CAP; ++spins_) { \
      if (far_sleep && __popcll(ma_) > 16) __builtin_amdgcn_s_sleep(far_sleep); \
      u64 sv_; \
      asm volatile("s_mov_b64 %1, exec\n\ts_mov_b64 exec, %2\n\tbuffer_load_dwordx4 %0, %3, %4, 0 offen " pol "\n\ts_mov_b64 exec, %1\n\ts_waitcnt vmcnt(0)" \
                   : "+v"(a), "=&s"(sv_) : "s"(ma_), "v"(o0), "s"(r) : "memory"); \
      ma_ &= BALLOT(!pair_ok(a, T)); \
      if (ma_ == 0) break; } } }
#define RETRY_SLEEP 0
#define SETTLE2S(r, o0, o1, T, a, b, slp, pol) SETTLE2G(r, o0, o1, T, a, b, slp, -1, pol)
#define SETTLE2(r, o0, o1, T, a, b, pol) SETTLE2G(r, o0, o1, T, a, b, 0, 0, pol)
#define SETTLE2G(r, o0, o1, T, a, b, far_sleep, thr, pol) \
  _Pragma("unroll 1") for (unsigned spins_ = 0; spins_ < SPIN_CAP; ++spins_) { \
    const u64 ma_ = BALLOT(!pair_ok(a, T)), mb_ = BALLOT(!pair_ok(b, T)); \
    if ((ma_ | mb_) == 0) break; \
    if (far_sleep && (int)(__popcll(ma_) + __popcll(mb_)) > (thr)) __builtin_amdgcn_s_sleep(far_sleep); \
    u64 sv_; \
    asm volatile("s_mov_b64 %2, exec\n\ts_mov_b64 exec, %3\n\tbuffer_load_dwordx4 %0, %5, %7, 0 offen " pol "\n\t" \
                 "s_mov_b64 exec, %4\n\tbuffer_load_dwordx4 %1, %6, %7, 0 offen " pol "\n\ts_mov_b64 exec, %2\n\ts_waitcnt vmcnt(0)" \
                 : "+v"(a), "+v"(b), "=&s"(sv_) : "s"(ma_), "s"(mb_), "v"(o0), "v"(o1), "s"(r) : "memory"); }
#define SETTLE4(r, o0, o1, o2, o3, T, a, b, c, d, pol) \
  _Pragma("unroll 1") for (unsigned spins_ = 0; spins_ < SPIN_CAP; ++spins_) { \
    const u64 m0_ = BALLOT(!(pair_ok(a, T) & pair_ok(b, T))), m1_ = BALLOT(!(pair_ok(c, T) & pair_ok(d, T))); \
    if ((m0_ | m1_) == 0) break; \
    u64 sv_; \
    asm volatile("s_mov_b64 %4, exec\n\ts_mov_b64 exec, %5\n\tbuffer_load_dwordx4 %0, %7, %11, 0 offen " pol "\n\tbuffer_load_dwordx4 %1, %8, %11, 0 offen " pol "\n\t" \
                 "s_mov_b64 exec, %6\n\tbuffer_load_dwordx4 %2, %9, %11, 0 offen " pol "\n\tbuffer_load_dwordx4 %3, %10, %11, 0 offen " pol "\n\t" \
                 "s_mov_b64 exec, %4\n\ts_waitcnt vmcnt(0)" \
                 : "+v"(a), "+v"(b), "+v"(c), "+v"(d), "=&s"(sv_) : "s"(m0_), "s"(m1_), "v"(o0), "v"(o1), "v"(o2), "v"(o3), "s"(r) : "memory"); }
__device__ __forceinline__ f32x4 rowmix(unsigned u2, u32x4 msk, u32x4 wreg, f32x4 acc) {
  const u32x4 a = {u2 & msk[0], u2 & msk[1], u2 & msk[2], u2 & msk[3]};
  return __builtin_amdgcn_mfma_f32_16x16x32_bf16(__builtin_bit_cast(s16x8, a), __builtin_bit_cast(s16x8, wreg), acc, 0, 0, 0);
}
__device__ __forceinline__ float fold32(float a, float b) {
  auto s = __builtin_amdgcn_permlane32_swap(asu(a), asu(b), false, false);
  return asf(s[0]) + asf(s[1]);
}
__device__ __forceinline__ float fold16(float a, float b) {
  auto s = __builtin_amdgcn_permlane16_swap(asu(a), asu(b), false, false);
  return asf(s[0]) + asf(s[1]);
}
struct Lds {
  unsigned sync[4];
  float qred[8][64];
  float qh[2][256];
  float hb[6][292];
  float ob[4][256];
  float ml[8][2];
  unsigned um[4][12];
  unsigned ux[8][6];
  float fin[4][4];
  float gt[2];
};

enum { S_ACT };
constexpr unsigned NOTYET = 0x0000ffffu;
typedef __attribute__((address_space(3))) unsigned lds_u32;
__device__ __forceinline__ void arrive(unsigned* c) {
  const unsigned a = (unsigned)(size_t)(lds_u32*)c;
  u64 sv;
  asm volatile("s_waitcnt lgkmcnt(0)\n\ts_mov_b64 %0, exec\n\ts_mov_b64 exec, 1\n\tds_add_u32 %1, %2\n\ts_mov_b64 exec, %0" : "=&s"(sv) : "v"(a), "v"(1u) : "memory");
}
template <int SLEEP> __device__ __forceinline__ void await(unsigned* c, unsigned n) {
#pragma unroll 1
  for (unsigned spins = 0; spins < (1u << 24); ++spins) {
    if (__builtin_amdgcn_readfirstlane(*(volatile lds_u32*)(lds_u32*)c) >= n) break;
    if (SLEEP) __builtin_amdgcn_s_sleep(SLEEP);
  }
  asm volatile("" ::: "memory");
}
#define BAR() asm volatile("s_waitcnt lgkmcnt(0)\n\ts_barrier" ::: "memory")

__global__ void __launch_bounds__(320, 2)
fused_decode(const u16* wq, const u16* x, unsigned char* ws, const u16* w1, const u16* kc, const u16* gw, const u16* vc,
             const u16*, const u16*, const u16*, const u16*, u16*) {
  __shared__ __attribute__((aligned(16))) Lds L;
  STAMP(0);
  u32x8 late; u32x2 late2;
  {
    const auto ka = __builtin_amdgcn_kernarg_segment_ptr();
    asm volatile("s_load_dwordx8 %0, %2, 0x38\n\ts_load_dwordx2 %1, %2, 0x58" : "=&s"(late), "=&s"(late2) : "s"(ka));
  }
  const int b = blockIdx.x, t = threadIdx.x, l = t & 63, w = __builtin_amdgcn_readfirstlane(t >> 6);
  const int h = ((b & 7) << 1) | ((b >> 3) & 1), s = b >> 4, gid = h * 16 + s;
#define XS 4
#define XQ 4
  const int hc = (h + 2 * XS) & 15, gidc = hc * 16 + s;
  const int cb = s & 3, rg = s >> 2;

  const int hq = (h + 2 * XQ) & 15;
  unsigned o_wq = (144 * rg) * (NQ * 2) + hq * 512 + cb * 128, o_kv = (h * SK + (SK - WIN) + s * 256) * (DH * 2);
  unsigned o_w1 = (36 * s) * (FF * 2) + h * 576, o_w2 = gidc * (18 * DM * 2), o_wo = gidc * (16 * DM * 2), o_b1 = gidc * 36;
  int wqo = (36 * w + 4 * (l >> 3)) * 8192 + (l & 7) * 16;
  asm volatile("" : "+s"(o_wq), "+s"(o_kv), "+s"(o_w1), "+s"(o_w2), "+s"(o_wo), "+s"(o_b1));
  TIE(wqo);
  asm volatile("" : "+s"(wq), "+s"(x), "+s"(ws), "+s"(w1), "+s"(kc), "+s"(gw), "+s"(vc));
  const srd_t rws = mk(ws, WS_END);
  const srd_t rx = mk(x, DM * 2), rgw = mk(gw, DM * 2);
  const srd_t rwq = mk((const char*)wq + o_wq, 143 * 8192 + 128);
  const srd_t rk = mk((const char*)kc + o_kv, 65536), rv = mk((const char*)vc + o_kv, 65536);
  const srd_t rw1 = mk((const char*)w1 + o_w1, 35 * 9216 + 576);
  if (w == 4) {
    __builtin_amdgcn_s_setprio(3);
    unsigned ep; u32x4 xa, xb, ga, gb;
    {
      const int o0 = l * 16, o1 = o0 + 1024, z = WS_EPOCH;
      LD1Z(ep, rws, z, "sc1"); LD4Z(xa, rx, o0, ""); LD4Z(xb, rx, o1, ""); LD4Z(ga, rgw, o0, ""); LD4Z(gb, rgw, o1, "");
    }
    asm volatile("s_waitcnt lgkmcnt(0)" : "+s"(late), "+s"(late2));
    if (l < 4) L.sync[l] = 0u;
    (&L.um[0][0])[l] = (l < 48 ? l % 12 < 4 : (l - 48) % 6 < 2) ? NOTYET : 0u;
    if (l < 32) (&L.um[0][0])[64 + l] = (l + 16) % 6 < 2 ? NOTYET : 0u;
    BAR();
    WAITV1(4, ep);
    unsigned T = __builtin_amdgcn_readfirstlane(ep) + 1u;
    T = T ? T : 1u;
    BAR();
#pragma unroll
    for (int r = 0; r < 2; ++r) {
      const int tt = min(l + 64 * r, 79), j = tt / 5, c0 = 18 * j + 4 * (tt - 5 * j);
      f32x2 a = *(const f32x2*)&L.hb[0][c0], c = *(const f32x2*)&L.hb[0][c0 + 2];
#pragma unroll
      for (int i = 1; i < 6; ++i) { a += *(const f32x2*)&L.hb[i][c0]; c += *(const f32x2*)&L.hb[i][c0 + 2]; }
      PUT_QUAD(rws, l + 64 * r < 80 ? WS_H + gid * H_ROW + (l + 64 * r) * 16 : OOB, T, a[0], a[1], c[0], c[1], POL_H_ST);
    }
    asm volatile("s_waitcnt vmcnt(2)" : "+v"(xa), "+v"(xb), "+v"(ga), "+v"(gb));
    float g = 0.f;
#pragma unroll
    for (int k = 0; k < 4; ++k) {
      const unsigned q0 = cvtpk(blo(xa[k]) * blo(ga[k]), bhi(xa[k]) * bhi(ga[k])), q1 = cvtpk(blo(xb[k]) * blo(gb[k]), bhi(xb[k]) * bhi(gb[k]));
      g += (blo(q0) + bhi(q0)) + (blo(q1) + bhi(q1));
    }
    g = rbf(xrow_sum(row_sum(g)));
    g = rbf(__builtin_amdgcn_rcpf(1.f + __expf(-g)));
    if (l == 0) *(f32x2*)L.gt = f32x2{g, rbf(1.f - g)};
    const int rho = l >> 4, ob_ = WS_O + (hc * 16 + (l >> 2)) * O_ROW;
    int oo = ob_ + s * 64 + (l & 3) * 16, om = ob_ + 1024 + ((s >> 3) * 4 + (l & 3)) * 16;
    const int row = 4 * (l & 3) + rho;
    int slot = (l & 8) ? 11 : (l & 4) ? 48 + (row & 7) * 6 + (row >> 3) : rho * 12 + (l & 3);
    TIE(oo); TIE(om); TIE(slot);
    BAR();
    BAR();
    {
      const f32x2 v = *(const f32x2*)&L.ml[l & 7][0];
      float M = fmaxf(v[0], dpp<XOR1>(v[0]));
      M = fmaxf(M, dpp<XOR2>(M));
      PUT_PAIR(rws, l < 8 ? WS_O + gid * O_ROW + 1024 + l * 16 : OOB, T, M, __builtin_amdgcn_exp2f(v[0] - M) * v[1], POL_ML_ST);
    }
    BAR();
    u32x4 p0, m0;
    LD4Z(m0, rws, om, POL_O_LD);
    __builtin_amdgcn_s_sleep(O_SLEEP);
    LD4Z(p0, rws, oo, POL_O_LD);
    WAITV1(1, m0);
    {
      const u64 bad = BALLOT(!pair_ok(m0, T));
      if (__builtin_expect(bad != 0, 0)) { WAITV1(0, p0); SETTLE1(rws, om, T, true, m0, 0, POL_O_LD); }
    }
    float M = asf(m0[0]);
    M = fmaxf(M, dpp<ROR4>(M)); M = fmaxf(M, dpp<ROR8>(M)); M = xrow_max(M);
    float e = __builtin_amdgcn_exp2f(asf(m0[0]) - M);
    e *= __builtin_amdgcn_rcpf(xrow_sum(row_sum(e * asf(m0[2]))));
    TIE(e);
    WAITV1(0, p0);
    SETTLE1(rws, oo, T, true, p0, 0, POL_O_LD);
    STAMP(8);
    float v = fold16(fold32(e * blo(p0[0]), e * blo(p0[2])), fold32(e * bhi(p0[0]), e * bhi(p0[2])));
    v += dpp<ROR4>(v);
    v += dpp<ROR8>(v);
    (&L.um[0][0])[slot] = cvtpk(v, v);
    if ((b >> 3) < 18) BAR();
    return;
  }
  unsigned epoch, xq4; u32x2 xq; u32x3 x1; u32x4 aq[5], a1[6];
  const int r6 = t / 36, c36 = t - r6 * 36;
  {
    const int g = l >> 3, xo = (144 * rg + 36 * w) * 2;
    const int o0 = xo + 8 * g, o1 = g < 4 ? xo + 64 + 2 * g : OOB, z = WS_EPOCH;
#pragma unroll
    for (int j = 0; j < 4; ++j) LD4(aq[j], rwq, wqo, j * 8192, "nt");
    const int v4 = g < 4 ? (36 * w + 32 + g) * 8192 + (l & 7) * 16 : OOB;
    LD4Z(aq[4], rwq, v4, "nt");
    LD2Z(xq, rx, o0, ""); LDHZ(xq4, rx, o1, ""); LD1Z(epoch, rws, z, "sc1");
    const int v1 = t < 216 ? r6 * 6 * 9216 + c36 * 16 : OOB, x1o = (36 * s + 6 * min(r6, 5)) * 2;
    LD3Z(x1, rx, x1o, "");
#pragma unroll
    for (int j = 0; j < 6; ++j) LD4(a1[j], rw1, v1, j * 9216, "nt");
  }
  asm volatile("s_waitcnt lgkmcnt(0)");
  PIN();
  u32x4 ak[16], av[16];
  const int kvoff = w * 1024 + ((l & 15) >> 2) * 256 + (l & 3) * 16 + (l >> 4) * 64;
  const int vvoff = w * 1024 + l * 16;
#pragma unroll
  for (int j = 0; j < K0; ++j) LD4(ak[j], rk, kvoff, j * 4096, "nt");
  u32x4 msk;
  {
    const unsigned half = (l & 1) ? 0xffff0000u : 0x0000ffffu;
#pragma unroll
    for (int k = 0; k < 4; ++k) { msk[k] = (((l & 7) >> 1) == k) ? half : 0u; TIE(msk[k]); }
  }
  int qpo = t < 32 ? WS_Q + (hq * 16 + s) * 512 + t * 16 : OOB;
  TIE(qpo);
  asm volatile("s_waitcnt lgkmcnt(0)" : "+s"(late), "+s"(late2));
#define PTR(lo, hi) ((const char*)(((u64)(hi) << 32) | (lo)))
  const srd_t rw2 = mk(PTR(late[0], late[1]) + o_w2, 18 * 1152), rwo = mk(PTR(late[2], late[3]) + o_wo, 16 * 1152);
  const srd_t rb1 = mk(PTR(late[4], late[5]) + o_b1, 36), rb2 = mk(PTR(late[6], late[7]), DM * 2), rout = mk(PTR(late2[0], late2[1]), DM * 2);
  PIN();
  STAMP(12);
  WAITN(7 + K0); TIE(epoch); TIE(xq); TIE(xq4); TIE(aq[0]); TIE(aq[1]); TIE(aq[2]); TIE(aq[3]); TIE(aq[4]);
  STAMP(1);
  unsigned T = __builtin_amdgcn_readfirstlane(epoch) + 1u;
  T = T ? T : 1u;
  {
    f32x4 acc = {0.f, 0.f, 0.f, 0.f};
    acc = rowmix(__builtin_amdgcn_perm(xq[0], xq[0], 0x01000100u), msk, aq[0], acc);
    acc = rowmix(__builtin_amdgcn_perm(xq[0], xq[0], 0x03020302u), msk, aq[1], acc);
    acc = rowmix(__builtin_amdgcn_perm(xq[1], xq[1], 0x01000100u), msk, aq[2], acc);
    acc = rowmix(__builtin_amdgcn_perm(xq[1], xq[1], 0x03020302u), msk, aq[3], acc);
    acc = rowmix(__builtin_amdgcn_perm(xq4, xq4, 0x01000100u), msk, aq[4], acc);
    if ((l >> 5) == ((l >> 3) & 1)) *(f32x4*)&L.qred[2 * w + (l >> 5)][8 * (l & 7) + 4 * ((l >> 4) & 1)] = acc;
  }
  BAR();
  {
    f32x2 a = *(const f32x2*)&L.qred[0][2 * (t & 31)];
#pragma unroll
    for (int i = 1; i < 8; ++i) a += *(const f32x2*)&L.qred[i][2 * (t & 31)];
    PUT_PAIR(rws, qpo, T, a[0], a[1], POL_Q_ST);
  }
  PIN();
  STAMP(2);
#pragma unroll
  for (int j = K0; j < K1; ++j) LD4(ak[j], rk, kvoff, j * 4096, "nt");
  PIN();
  {
    float acc[8] = {0.f, 0.f, 0.f, 0.f, 0.f, 0.f, 0.f, 0.f};
    WAITN(K1 + 1); TIE(x1);
#pragma unroll
    for (int j = 0; j < 6; ++j) TIE(a1[j]);
#pragma unroll
    for (int j = 0; j < 6; ++j) fma8(acc, a1[j], (j & 1) ? bhi(x1[j >> 1]) : blo(x1[j >> 1]));
    if (t < 216) {
      *(f32x4*)&L.hb[r6][8 * c36] = f32x4{acc[0], acc[1], acc[2], acc[3]};
      *(f32x4*)&L.hb[r6][8 * c36 + 4] = f32x4{acc[4], acc[5], acc[6], acc[7]};
    }
  }
  BAR();
  STAMP(15);
#pragma unroll
  for (int j = K1; j < 16; ++j) LD4(ak[j], rk, kvoff, j * 4096, "nt");
  unsigned b1v, b2v;
  const int fin = (b & 7) * 18 + (b >> 3);
  const int ocol = (fin < 128 ? (fin >> 5) * 128 + (fin & 15) * 8 + ((fin >> 4) & 1) * 4 : 512 + (fin & 7) * 8 + ((fin >> 3) & 1) * 4) + (t & 3);
  {
    const int bo = t >= 64 ? (((t - 64) >> 4) * 4 + (t & 3)) * 2 : OOB, b2o = (b >> 3) < 18 ? ocol * 2 : OOB;
    LDHZ(b1v, rb1, bo, ""); LDHZ(b2v, rb2, b2o, "");
  }
  const int qo0 = WS_Q + (h * 16 + (t >> 7) * 8 + ((t & 127) >> 5)) * 512 + (t & 31) * 16, qo1 = qo0 + 4 * 512;
  u32x4 pa, pb;
#pragma unroll
  for (int j = 0; j < QP; ++j) LD4(av[j], rv, vvoff, j * 4096, "nt");
  LD4Z(pa, rws, qo0, POL_Q_LD); LD4Z(pb, rws, qo1, POL_Q_LD);
#pragma unroll
  for (int j = QP; j < QB; ++j) LD4(av[j], rv, vvoff, j * 4096, "nt");
  u32x4 pc, pd;
  LD4Z(pc, rws, qo0, POL_Q_LD); LD4Z(pd, rws, qo1, POL_Q_LD);
#pragma unroll
  for (int j = QB; j < VPRE; ++j) LD4(av[j], rv, vvoff, j * 4096, "nt");
  PIN();
  STAMP(3);
  u32x4 a2[5], ao[4], a2x[3], aox[2];
  const int rho = l >> 4;
  const int wvo = rho * 1152 + w * 256 + (l & 15) * 16;
  int x2o_ = w == 3 ? (l >> 3) * 1152 + 1024 + (l & 7) * 16 : OOB;
  constexpr int NV = 16 - VPRE, NREST = NV + 14, NPRE = N1 + N2 + N3 + N4 + N5;
  static_assert(NPRE >= NV && NPRE <= NREST && NREST - NPRE <= 16, "V is all requested before P.V; one load per P.V step is enough for the rest");
#define LDR(k) do { constexpr int i_ = (k) - NV; \
    if ((k) < NV) LD4(av[(k) < NV ? VPRE + (k) : 0], rv, vvoff, (VPRE + (k)) * 4096, "nt"); \
    else if (i_ < 5) LD4(a2[i_ >= 0 && i_ < 5 ? i_ : 0], rw2, wvo, i_ * 4608, "nt"); \
    else if (i_ < 8) LD4(a2x[i_ >= 5 && i_ < 8 ? i_ - 5 : 0], rw2, x2o_, (i_ - 5) * 9216, "nt"); \
    else if (i_ < 12) LD4(ao[i_ >= 8 && i_ < 12 ? i_ - 8 : 0], rwo, wvo, (i_ - 8) * 4608, "nt"); \
    else if (i_ < 14) LD4(aox[i_ >= 12 && i_ < 14 ? i_ - 12 : 0], rwo, x2o_, (i_ - 12) * 9216, "nt"); } while (0)
#define LDRS(a, n) do { if ((n) > 0) LDR(a); if ((n) > 1) LDR((a) + 1); if ((n) > 2) LDR((a) + 2); if ((n) > 3) LDR((a) + 3); PIN(); } while (0)
  LDRS(0, N1);
  const int th_ = t - 64;
  const bool hl = th_ >= 0 && th_ < 80;
  int ho = hl ? WS_H + (hc * 16 + (t & 15)) * H_ROW + (s * 5 + (th_ >> 4)) * 16 : OOB;
  TIE(ho);
  int opo = WS_O + gid * O_ROW + l * 16;
  int obw = (w * 256 + (l >> 5) * 128 + (l & 15) * 8 + ((l >> 4) & 1) * 4) * 4;
  const int row = 4 * (th_ >> 4) + (l & 3);
  int slot = (l & 4) ? 48 + (row & 7) * 6 + (row >> 3) + 2 : (row & 3) * 12 + (row >> 2) + 4;
  slot = (l & 8) == 0 && hl && row < 18 ? slot : 10;
  int yo = l < 32 ? WS_Y + b * Y_ROW + (32 * w + l) * 16 : OOB;
  int yxo = (l & 40) == 0 ? WS_Y + b * Y_ROW + (128 + 8 * rho + (l & 7)) * 16 : OOB;
  int fo = WS_Y + t * Y_ROW + fin * 16, oc2 = ocol * 2;
  TIE(opo); TIE(obw); TIE(slot); TIE(yo); TIE(yxo); TIE(fo); TIE(oc2);
  TIE(x2o_);
  PIN();
  WAITN(VPRE - QP + 2 + N1); TIE(pa); TIE(pb); TIE(b1v); TIE(b2v);
#pragma unroll
  for (int j = 0; j < 16; ++j) TIE(ak[j]);
  if (BALLOT(!(pair_ok(pa, T) & pair_ok(pb, T))) != 0) {
    WAITN(VPRE - QB + N1); TIE(pc); TIE(pd);
    pa = pc; pb = pd;
    SETTLE2(rws, qo0, qo1, T, pa, pb, POL_Q_LD);
  }
  *(f32x2*)&L.qh[t >> 7][2 * (t & 127)] = f32x2{asf(pa[0]) + asf(pb[0]), asf(pa[2]) + asf(pb[2])};
  BAR();
  u32x4 ph;
  LD4Z(ph, rws, ho, POL_H_LD);
  static_assert(N1 < 16 - VPRE, "the look at h goes out in front of the last V load");
  LDRS(N1, N2);
  STAMP(4);
  u32x4 qf;
  {
    const int o = ((l >> 3) & 1) * 128 + ((l & 3) + 4 * (l >> 4)) * 8;
    const f32x4 a0 = *(const f32x4*)&L.qh[0][o] + *(const f32x4*)&L.qh[1][o];
    const f32x4 a1_ = *(const f32x4*)&L.qh[0][o + 4] + *(const f32x4*)&L.qh[1][o + 4];
    qf = u32x4{cvtpk(a0[0], a0[1]), cvtpk(a0[2], a0[3]), cvtpk(a1_[0], a1_[1]), cvtpk(a1_[2], a1_[3])};
  }
  constexpr float C2 = 0.08838834764831845f * 1.4426950408889634f;
  float sc[16];
  {
    f32x4 d[16];
#pragma unroll
    for (int j = 0; j < 16; ++j)
      d[j] = __builtin_amdgcn_mfma_f32_16x16x32_bf16(__builtin_bit_cast(s16x8, ak[j]), __builtin_bit_cast(s16x8, qf), f32x4{0.f, 0.f, 0.f, 0.f}, 0, 0, 0);
    PIN();
    LDRS(N1 + N2, N3);
#pragma unroll
    for (int j = 0; j < 16; ++j) sc[j] = (dpp<0x00>(d[j][0]) + dpp<0x55>(d[j][1])) + (dpp<0xAA>(d[j][2]) + dpp<0xFF>(d[j][3]));
  }
  PIN();
  LDRS(N1 + N2 + N3, N4);
  float mx = -INFINITY;
#pragma unroll
  for (int j = 0; j < 16; ++j) mx = fmaxf(mx, sc[j]);
  mx = xrow_max(mx) * C2;
  float lsum = 0.f;
  unsigned pk[16];
#pragma unroll
  for (int j = 0; j < 16; ++j) { const float p = __builtin_amdgcn_exp2f(fmaf(sc[j], C2, -mx)); lsum += p; pk[j] = cvtpk(p, p); }
  lsum = xrow_sum(lsum);
  if ((l & 55) == 0) *(f32x2*)&L.ml[(l >> 3) * 4 + w][0] = f32x2{mx, lsum};
  PIN();
  LDRS(N1 + N2 + N3 + N4, N5);
  STAMP(5);
  float gate = L.gt[0], ngate = L.gt[1];
  TIE(gate); TIE(ngate);
  f32x4 oacc = {0.f, 0.f, 0.f, 0.f};
#define PV_STEP(j) { WAITN(15 - (j) + (NPRE - NV) + ((j) < NREST - NPRE ? (j) : NREST - NPRE)); TIE(av[j]); \
    oacc = rowmix(pk[j], msk, av[j], oacc); PIN(); LDR(NPRE + (j)); }
  PV_STEP(0) PV_STEP(1) PV_STEP(2) PV_STEP(3)
  BAR();
  float osc;
  {
    const f32x2 v = *(const f32x2*)&L.ml[l & 7][0];
    float M = fmaxf(v[0], dpp<XOR1>(v[0]));
    M = fmaxf(M, dpp<XOR2>(M));
    const float e = __builtin_amdgcn_exp2f(v[0] - M);
    const float c0_ = asf(__builtin_amdgcn_readlane(asu(e), w)), c1_ = asf(__builtin_amdgcn_readlane(asu(e), 4 + w));
    osc = l < 32 ? c0_ : c1_;
    TIE(osc);
  }
  PIN();
  PV_STEP(4) PV_STEP(5) PV_STEP(6) PV_STEP(7) PV_STEP(8) PV_STEP(9) PV_STEP(10) PV_STEP(11) PV_STEP(12) PV_STEP(13)
  TIE(pc); TIE(pd);
  PV_STEP(14) PV_STEP(15)
  TIE(ph);
  *(f32x4*)((char*)&L.ob[0][0] + obw) = oacc * osc;
  STAMP(6);
  BAR();
  if (w == 3) {
    const f32x4 v = (*(const f32x4*)&L.ob[0][4 * l] + *(const f32x4*)&L.ob[1][4 * l]) + (*(const f32x4*)&L.ob[2][4 * l] + *(const f32x4*)&L.ob[3][4 * l]);
    PUT_QUAD(rws, opo, T, v[0], v[1], v[2], v[3], POL_O_ST);
  }
  PIN();
  STAMP(7);
  {
    if (w == 1 || w == 2) {
      u32x4 p0 = ph;
      SETTLE1(rws, ho, T, hl, p0, 0, POL_H_LD);
      const float s0 = row_sum(blo(p0[0])), s1 = row_sum(bhi(p0[0])), s2 = row_sum(blo(p0[2])), s3 = row_sum(bhi(p0[2]));
      const float sv = (t & 2) ? ((t & 1) ? s3 : s2) : ((t & 1) ? s1 : s0);
      const float hv = rbf(rbf(sv) + blo(b1v));
      const float z = 0.7978845608028654f * (hv + 0.044715f * hv * hv * hv);
      const float th = 1.f - 2.f * __builtin_amdgcn_rcpf(1.f + __expf(2.f * z));
      const float v = 0.5f * hv * (1.f + th);
      (&L.um[0][0])[slot] = cvtpk(v, v);
      arrive(&L.sync[S_ACT]);
    }
    await<0>(&L.sync[S_ACT], 2);
    STAMP(13);
    if (w == 3) WAITN(7); else WAITN(6);
#pragma unroll
    for (int j = 0; j < 5; ++j) TIE(a2[j]);
    TIE(a2x[0]); TIE(a2x[1]); TIE(a2x[2]);
    const f32x4 zero = {0.f, 0.f, 0.f, 0.f};
    const unsigned* ux = &L.ux[l >> 3][0];
    const u32x4 ua = *(const u32x4*)&L.um[rho][4];
    const unsigned ua4 = L.um[rho][8];
    f32x4 yb = rowmix(ua4, msk, a2[4], rowmix(ua[3], msk, a2[3], rowmix(ua[2], msk, a2[2], rowmix(ua[1], msk, a2[1], rowmix(ua[0], msk, a2[0], zero))))) * ngate;
    f32x4 xb = zero;
    if (w == 3) xb = rowmix(ux[4], msk, a2x[2], rowmix(ux[3], msk, a2x[1], rowmix(ux[2], msk, a2x[0], zero))) * ngate;
    TIE(yb); TIE(xb);
    PIN();
    if (w == 3) WAITN(1); else WAITN(0);
#pragma unroll
    for (int j = 0; j < 4; ++j) TIE(ao[j]);
    TIE(aox[0]); TIE(aox[1]);
    STAMP(14);
    u32x4 uo; u32x2 uox;
    {
      const unsigned a0 = (unsigned)(size_t)(lds_u32*)&L.um[rho][0], a1_ = (unsigned)(size_t)(lds_u32*)ux;
#pragma unroll 1
      for (unsigned spins = 0; spins < (1u << 22); ++spins) {
        asm volatile("ds_read_b128 %0, %2\n\tds_read_b64 %1, %3\n\ts_waitcnt lgkmcnt(0)" : "=&v"(uo), "=&v"(uox) : "v"(a0), "v"(a1_) : "memory");
        if (BALLOT((uo[0] == NOTYET) | (uo[1] == NOTYET) | (uo[2] == NOTYET) | (uo[3] == NOTYET) | (uox[0] == NOTYET) | (uox[1] == NOTYET)) == 0) break;
      }
    }
    const f32x4 ya = rowmix(uo[1], msk, ao[1], rowmix(uo[0], msk, ao[0], zero)) + rowmix(uo[3], msk, ao[3], rowmix(uo[2], msk, ao[2], zero));
    const f32x4 y = gate * ya + yb;
    PUT_QUAD(rws, yo, T, y[0], y[1], y[2], y[3], POL_Y_ST);
    if (w == 3) {
      f32x4 z = gate * (rowmix(uox[0], msk, aox[0], zero) + rowmix(uox[1], msk, aox[1], zero)) + xb;
#pragma unroll
      for (int k = 0; k < 4; ++k) {
        const float r = asf(__builtin_amdgcn_update_dpp((int)asu(z[k]), (int)asu(z[k]), ROR8, 0xC, 0xF, false));
        z[k] = fold32(r, r);
      }
      PUT_QUAD(rws, yxo, T, z[0], z[1], z[2], z[3], POL_Y_ST);
    }
  }
  PIN();
  STAMP(9);
  if ((b >> 3) < 18) {
    __builtin_amdgcn_s_sleep(FIN_SLEEP);
    LD4Z(pa, rws, fo, POL_Y_LD);
    WAITV1(0, pa);
    SETTLE1(rws, fo, T, true, pa, FAR_SLEEP_3, POL_Y_LD);
    STAMP(10);
    const float v = row_sum(fold16(fold32(blo(pa[0]), blo(pa[2])), fold32(bhi(pa[0]), bhi(pa[2]))));
    if ((l & 15) == 0) L.fin[rho][w] = v;
    BAR();
    if (t < 4) {
      const f32x4 f = *(const f32x4*)&L.fin[t][0];
      const float r = ((f[0] + f[1]) + (f[2] + f[3])) + ngate * blo(b2v);
      const unsigned pr = cvtpk(r, r);
      STHZ(pr, rout, oc2, "");
    }
    if (b == 0 && t == 0) { const int z = WS_EPOCH; const unsigned tv = T; ST1Z(tv, rws, z, ""); }
  }
}

extern "C" void kernel_launch(void* const* d_in, const int* in_sizes, int n_in, void* d_out, int out_size, void* d_ws, size_t ws_size, hipStream_t stream) {
  fused_decode<<<256, 320, 0, stream>>>((const u16*)d_in[3], (const u16*)d_in[0], (unsigned char*)d_ws, (const u16*)d_in[6], (const u16*)d_in[1],
                                        (const u16*)d_in[10], (const u16*)d_in[2], (const u16*)d_in[8], (const u16*)d_in[4], (const u16*)d_in[7],
                                        (const u16*)d_in[9], (u16*)d_out);
}
```

```cpp
#include <hip/hip_runtime.h>

typedef unsigned short u16;
typedef unsigned u32x4 __attribute__((ext_vector_type(4)));
typedef unsigned u32x3 __attribute__((ext_vector_type(3)));
typedef unsigned u32x2 __attribute__((ext_vector_type(2)));
typedef unsigned u32x8 __attribute__((ext_vector_type(8)));
typedef unsigned u32x16 __attribute__((ext_vector_type(16)));
typedef float f32x4 __attribute__((ext_vector_type(4)));
typedef float f32x2 __attribute__((ext_vector_type(2)));
typedef short s16x4 __attribute__((ext_vector_type(4)));
typedef short s16x8 __attribute__((ext_vector_type(8)));

constexpr int DM = 576, SK = 32768, DH = 128, WIN = 4096, FF = 4608, NQ = 4096;
constexpr int OOB = 0x40000000;

constexpr unsigned WS_EPOCH = 0;
constexpr unsigned WS_Q = 4096;
constexpr unsigned H_ROW = 1280, O_ROW = 1152, Y_ROW = 2304;
constexpr unsigned WS_H = WS_Q + 256 * 512;
constexpr unsigned WS_O = WS_H + 256 * H_ROW;
constexpr unsigned WS_Y = WS_O + 256 * O_ROW;
constexpr unsigned WS_END = WS_Y + 256 * Y_ROW;
constexpr unsigned SPIN_CAP = 1u << 18;

#define POL_EARLY "sc1 nt"
#define POL_LATE "sc1"

constexpr int KA = 3;
constexpr int K0 = 7;
constexpr int K1 = 12;
constexpr int WND = 12;
constexpr int QP = 6;
constexpr int QB = 11;
constexpr int N4 = 0;
constexpr int N5 = 0;
constexpr int WD = 2;
constexpr int O_SLEEP = 12;
constexpr int FIN_SLEEP = 25;
constexpr int FAR_SLEEP = 4;

#define PIN() __builtin_amdgcn_sched_barrier(0)

typedef u32x4 srd_t;
__device__ __forceinline__ srd_t mk(const void* p, unsigned bytes) {
  const unsigned long long a = (unsigned long long)p;
  return srd_t{(unsigned)a, (unsigned)(a >> 32), bytes, 0x00020000u};
}
#define LD4(dst, srd, voff, soff, pol) asm volatile("buffer_load_dwordx4 %0, %1, %2, %3 offen " pol : "=v"(dst) : "v"(voff), "s"(srd), "s"(soff))
#define LD4Z(dst, srd, voff, pol) asm volatile("buffer_load_dwordx4 %0, %1, %2, 0 offen " pol : "=v"(dst) : "v"(voff), "s"(srd))
#define LD3Z(dst, srd, voff, pol) asm volatile("buffer_load_dwordx3 %0, %1, %2, 0 offen " pol : "=v"(dst) : "v"(voff), "s"(srd))
#define LD2Z(dst, srd, voff, pol) asm volatile("buffer_load_dwordx2 %0, %1, %2, 0 offen " pol : "=v"(dst) : "v"(voff), "s"(srd))
#define LDHZ(dst, srd, voff, pol) asm volatile("buffer_load_ushort %0, %1, %2, 0 offen " pol : "=v"(dst) : "v"(voff), "s"(srd))
#define ST4Z(val, srd, voff, pol) asm volatile("buffer_store_dwordx4 %0, %1, %2, 0 offen " pol "\n\ts_nop 1" :: "v"(val), "v"(voff), "s"(srd) : "memory")
#define ST1Z(val, srd, voff, pol) asm volatile("buffer_store_dword %0, %1, %2, 0 offen " pol :: "v"(val), "v"(voff), "s"(srd) : "memory")
#define STHZ(val, srd, voff, pol) asm volatile("buffer_store_short %0, %1, %2, 0 offen " pol :: "v"(val), "v"(voff), "s"(srd) : "memory")
#define WAITV1(n, a) asm volatile("s_waitcnt vmcnt(" #n ")" : "+v"(a))
#define WAITN(n) asm volatile("s_waitcnt vmcnt(%0)" :: "i"(n))
#define TIE(a) asm volatile("" : "+v"(a))
__device__ __forceinline__ float asf(unsigned u) { return __builtin_bit_cast(float, u); }
__device__ __forceinline__ unsigned asu(float f) { return __builtin_bit_cast(unsigned, f); }
__device__ __forceinline__ float blo(unsigned v) { return asf(v << 16); }
__device__ __forceinline__ float bhi(unsigned v) { return asf(v & 0xffff0000u); }
typedef __bf16 bf16x2_t __attribute__((ext_vector_type(2)));
__device__ __forceinline__ unsigned cvtpk(float lo, float hi) {
  return __builtin_bit_cast(unsigned, __builtin_convertvector(f32x2{lo, hi}, bf16x2_t));
}
__device__ __forceinline__ float rbf(float f) { return blo(cvtpk(f, f)); }
template <int CTRL> __device__ __forceinline__ float dpp(float x) {
  return asf(__builtin_amdgcn_mov_dpp((int)asu(x), CTRL, 0xf, 0xf, true));
}
constexpr int XOR1 = 0xB1, XOR2 = 0x4E, HMIR = 0x141, ROR8 = 0x128, ROR4 = 0x124;
__device__ __forceinline__ float xrow_sum(float x) {
  auto s = __builtin_amdgcn_permlane16_swap(asu(x), asu(x), false, false);
  x = asf(s[0]) + asf(s[1]);
  auto t = __builtin_amdgcn_permlane32_swap(asu(x), asu(x), false, false);
  return asf(t[0]) + asf(t[1]);
}
__device__ __forceinline__ float xrow_max(float x) {
  auto s = __builtin_amdgcn_permlane16_swap(asu(x), asu(x), false, false);
  x = fmaxf(asf(s[0]), asf(s[1]));
  auto t = __builtin_amdgcn_permlane32_swap(asu(x), asu(x), false, false);
  return fmaxf(asf(t[0]), asf(t[1]));
}
__device__ __forceinline__ float row_sum(float x) {
  x += dpp<XOR1>(x); x += dpp<XOR2>(x); x += dpp<HMIR>(x); x += dpp<ROR8>(x); return x;
}
__device__ __forceinline__ void fma8(float (&acc)[8], u32x4 w, float x) {
#pragma unroll
  for (int k = 0; k < 4; ++k) { acc[2 * k] = fmaf(x, blo(w[k]), acc[2 * k]); acc[2 * k + 1] = fmaf(x, bhi(w[k]), acc[2 * k + 1]); }
}
#define PUT_QUAD(r, voff, T, a, b, c, d, pol) PUT_PAIR(r, voff, T, asf(cvtpk(a, b)), asf(cvtpk(c, d)), pol)
#define PUT_PAIR(r, voff, T, a, b, pol) do { const float a_ = (a), b_ = (b); const int o_ = (voff); \
    const u32x4 v_ = {asu(a_), (T) ^ asu(b_), asu(b_), (T) ^ asu(a_)}; ST4Z(v_, r, o_, pol); } while (0)
__device__ __forceinline__ bool pair_ok(u32x4 v, unsigned T) { return ((v[1] ^ v[2]) == T) & ((v[3] ^ v[0]) == T); }
typedef unsigned long long u64;
#define BALLOT(c) __builtin_amdgcn_ballot_w64(c)
#define SETTLE(r, o0, T, live, a, far_sleep, pol) { \
  u64 ma_ = BALLOT((live) & !pair_ok(a, T)); \
  if (__builtin_expect(ma_ != 0, 0)) { \
    _Pragma("unroll 1") for (unsigned spins_ = 0; spins_ < SPIN_CAP; ++spins_) { \
      if (far_sleep && __popcll(ma_) > 16) __builtin_amdgcn_s_sleep(far_sleep); \
      u64 sv_; \
      asm volatile("s_mov_b64 %1, exec\n\ts_mov_b64 exec, %2\n\tbuffer_load_dwordx4 %0, %3, %4, 0 offen " pol "\n\ts_mov_b64 exec, %1\n\ts_waitcnt vmcnt(0)" \
                   : "+v"(a), "=&s"(sv_) : "s"(ma_), "v"(o0), "s"(r) : "memory"); \
      ma_ &= BALLOT(!pair_ok(a, T)); \
      if (ma_ == 0) break; } } }
__device__ __forceinline__ f32x4 rowmix(unsigned u2, u32x4 msk, u32x4 wreg, f32x4 acc) {
  const u32x4 a = {u2 & msk[0], u2 & msk[1], u2 & msk[2], u2 & msk[3]};
  return __builtin_amdgcn_mfma_f32_16x16x32_bf16(__builtin_bit_cast(s16x8, a), __builtin_bit_cast(s16x8, wreg), acc, 0, 0, 0);
}
__device__ __forceinline__ float fold32(float a, float b) {
  auto s = __builtin_amdgcn_permlane32_swap(asu(a), asu(b), false, false);
  return asf(s[0]) + asf(s[1]);
}
__device__ __forceinline__ float fold16(float a, float b) {
  auto s = __builtin_amdgcn_permlane16_swap(asu(a), asu(b), false, false);
  return asf(s[0]) + asf(s[1]);
}
__device__ __forceinline__ float rot8_hi(float x) { return asf(__builtin_amdgcn_update_dpp((int)asu(x), (int)asu(x), ROR8, 0xC, 0xF, false)); }
struct Lds {
  unsigned sync[4];
  float qred[8][64];
  u16 qs[256];
  float hb[6][292];
  float ob[4][256];
  float ml[8][2];
  unsigned um[4][12];
  unsigned ux[8][6];
  float fin[5][4];
  float gt[2];
};

enum { S_ACT };
constexpr unsigned NOTYET = 0xfffffffeu;
typedef __attribute__((address_space(3))) unsigned lds_u32;
__device__ __forceinline__ void arrive(unsigned* c) {
  const unsigned a = (unsigned)(size_t)(lds_u32*)c;
  u64 sv;
  asm volatile("s_waitcnt lgkmcnt(0)\n\ts_mov_b64 %0, exec\n\ts_mov_b64 exec, 1\n\tds_add_u32 %1, %2\n\ts_mov_b64 exec, %0" : "=&s"(sv) : "v"(a), "v"(1u) : "memory");
}
template <int SLEEP> __device__ __forceinline__ void await(unsigned* c, unsigned n) {
#pragma unroll 1
  for (unsigned spins = 0; spins < (1u << 21); ++spins) {
    if (__builtin_amdgcn_readfirstlane(*(volatile lds_u32*)(lds_u32*)c) >= n) break;
    if (SLEEP) __builtin_amdgcn_s_sleep(SLEEP);
  }
  asm volatile("" ::: "memory");
}
#define BAR() asm volatile("s_waitcnt lgkmcnt(0)\n\ts_barrier" ::: "memory")

__global__ void __launch_bounds__(320, 2)
fused_decode(const u16*, const u16*, unsigned char*, const u16*, const u16*, const u16*, const u16*,
             const u16*, const u16*, const u16*, const u16*, u16*) {
  __shared__ __attribute__((aligned(16))) Lds L;
  u32x16 ka; u32x8 kb;
  asm volatile("s_load_dwordx16 %0, %2, 0x0\n\ts_load_dwordx8 %1, %2, 0x40" : "=&s"(ka), "=&s"(kb) : "s"(__builtin_amdgcn_kernarg_segment_ptr()));
  const int b = blockIdx.x, t = threadIdx.x, l = t & 63, w = __builtin_amdgcn_readfirstlane(t >> 6);
  const int h = ((b & 7) << 1) | ((b >> 3) & 1), s = b >> 4, gid = h * 16 + s;
  const int hc = h ^ 8, gidc = hc * 16 + s, cb = s & 3, rg = s >> 2;
  unsigned o_wq = (144 * rg) * (NQ * 2) + hc * 512 + cb * 128, o_kv = (h * SK + (SK - WIN) + s * 256) * (DH * 2);
  unsigned o_w1 = (36 * s) * (FF * 2) + h * 576, o_w2 = gidc * (18 * DM * 2), o_wo = gidc * (16 * DM * 2), o_b1 = gidc * 36;
  int wqo = (36 * w + 4 * (l >> 3)) * 8192 + (l & 7) * 16;
  int wq4 = ((36 * w + 32 + (l >> 3)) * 8192 + (l & 7) * 16) | ((l >> 5) << 30);
  asm volatile("" : "+s"(o_wq), "+s"(o_kv), "+s"(o_w1), "+s"(o_w2), "+s"(o_wo), "+s"(o_b1));
  TIE(wqo); TIE(wq4);
  asm volatile("s_waitcnt lgkmcnt(0)" : "+s"(ka), "+s"(kb));
#define PTR(v, i) ((const char*)(((u64)v[2 * (i) + 1] << 32) | v[2 * (i)]))
  const char *wq = PTR(ka, 0), *x = PTR(ka, 1), *w1 = PTR(ka, 3), *kc = PTR(ka, 4), *gw = PTR(ka, 5), *vc = PTR(ka, 6), *w2 = PTR(ka, 7);
  const char *wo = PTR(kb, 0), *b1 = PTR(kb, 1), *b2 = PTR(kb, 2), *out = PTR(kb, 3);
  unsigned char* ws = (unsigned char*)PTR(ka, 2);
  u32x4 aq[5];
  {
    const srd_t r = mk(wq + o_wq, 143 * 8192 + 128);
#pragma unroll
    for (int j = 0; j < 4; ++j) LD4(aq[j], r, wqo, j * 8192, "nt");
    LD4Z(aq[4], r, wq4, "nt");
  }
  unsigned epoch;
  asm volatile("s_load_dword %0, %1, 0x0" : "=s"(epoch) : "s"(ws));
  PIN();
  const srd_t rws = mk(ws, WS_END);
  const srd_t rx = mk(x, DM * 2), rgw = mk(gw, DM * 2);
  const srd_t rw2 = mk(w2 + o_w2, 18 * 1152), rwo = mk(wo + o_wo, 16 * 1152);
  const srd_t rb1 = mk(b1 + o_b1, 36), rb2 = mk(b2, DM * 2), rout = mk(out, DM * 2);
  const srd_t rk = mk(kc + o_kv, 65536), rv = mk(vc + o_kv, 65536);
  const srd_t rw1 = mk(w1 + o_w1, 35 * 9216 + 576);
  if (w == 4) {
    __builtin_amdgcn_s_setprio(3);
    u32x4 xa, xb, ga, gb;
    {
      const int o0 = l * 16, o1 = o0 + 1024;
      LD4Z(xa, rx, o0, ""); LD4Z(xb, rx, o1, ""); LD4Z(ga, rgw, o0, ""); LD4Z(gb, rgw, o1, "");
    }
    asm volatile("s_waitcnt lgkmcnt(0)"); PIN(); asm volatile("" : "+s"(epoch));
    const unsigned T = epoch + 1u ? epoch + 1u : 1u;
    if (l < 4) L.sync[l] = 0u;
    (&L.um[0][0])[l] = (l < 48 ? l % 12 < 4 : (l - 48) % 6 < 2) ? NOTYET : 0u;
    if (l < 32) (&L.um[0][0])[64 + l] = (l + 16) % 6 < 2 ? NOTYET : 0u;
    WAITN(4); TIE(aq[0]); TIE(aq[1]); TIE(aq[2]); TIE(aq[3]); TIE(aq[4]);
    int qpo = l < 32 ? WS_Q + gidc * 512 + l * 16 : OOB;
    TIE(qpo);
    BAR();
    {
      const float* p = &L.qred[4 * (l >> 5)][2 * (l & 31)];
      const f32x2 a = (*(const f32x2*)p + *(const f32x2*)(p + 64)) + (*(const f32x2*)(p + 128) + *(const f32x2*)(p + 192));
      PUT_PAIR(rws, qpo, T, fold32(a[0], a[0]), fold32(a[1], a[1]), POL_LATE);
    }
    BAR();
#pragma unroll
    for (int r = 0; r < 2; ++r) {
      const int tt = min(l + 64 * r, 79), j = tt / 5, c0 = 18 * j + 4 * (tt - 5 * j);
      f32x2 a = *(const f32x2*)&L.hb[0][c0], c = *(const f32x2*)&L.hb[0][c0 + 2];
#pragma unroll
      for (int i = 1; i < 6; ++i) { a += *(const f32x2*)&L.hb[i][c0]; c += *(const f32x2*)&L.hb[i][c0 + 2]; }
      PUT_QUAD(rws, l + 64 * r < 80 ? WS_H + gid * H_ROW + (l + 64 * r) * 16 : OOB, T, a[0], a[1], c[0], c[1], POL_EARLY);
    }
    asm volatile("s_waitcnt vmcnt(3)" : "+v"(xa), "+v"(xb), "+v"(ga), "+v"(gb));
    float g = 0.f;
#pragma unroll
    for (int k = 0; k < 4; ++k) {
      const unsigned q0 = cvtpk(blo(xa[k]) * blo(ga[k]), bhi(xa[k]) * bhi(ga[k])), q1 = cvtpk(blo(xb[k]) * blo(gb[k]), bhi(xb[k]) * bhi(gb[k]));
      g += (blo(q0) + bhi(q0)) + (blo(q1) + bhi(q1));
    }
    g = rbf(xrow_sum(row_sum(g)));
    g = rbf(__builtin_amdgcn_rcpf(1.f + __expf(-g)));
    if (l == 0) *(f32x2*)L.gt = f32x2{g, rbf(1.f - g)};
    const int rho = l >> 4, ob_ = WS_O + (hc * 16 + (l >> 2)) * O_ROW;
    int oo = ob_ + s * 64 + (l & 3) * 16, om = ob_ + 1024 + ((s >> 3) * 4 + (l & 3)) * 16;
    const int row = 4 * (l & 3) + rho;
    int slot = (l & 8) ? 11 : (l & 4) ? 48 + (row & 7) * 6 + (row >> 3) : rho * 12 + (l & 3);
    TIE(oo); TIE(om); TIE(slot);
    BAR();
    BAR();
    {
      const f32x2 v = *(const f32x2*)&L.ml[l & 7][0];
      float M = fmaxf(v[0], dpp<XOR1>(v[0]));
      M = fmaxf(M, dpp<XOR2>(M));
      PUT_PAIR(rws, l < 8 ? WS_O + gid * O_ROW + 1024 + l * 16 : OOB, T, M, __builtin_amdgcn_exp2f(v[0] - M) * v[1], POL_LATE);
    }
    BAR();
    u32x4 p0, m0;
    LD4Z(m0, rws, om, POL_LATE);
    __builtin_amdgcn_s_sleep(O_SLEEP);
    LD4Z(p0, rws, oo, POL_LATE);
    WAITV1(1, m0);
    {
      const u64 bad = BALLOT(!pair_ok(m0, T));
      if (__builtin_expect(bad != 0, 0)) { WAITV1(0, p0); SETTLE(rws, om, T, true, m0, 0, POL_LATE); }
    }
    float M = asf(m0[0]);
    M = fmaxf(M, dpp<ROR4>(M)); M = fmaxf(M, dpp<ROR8>(M)); M = xrow_max(M);
    float e = __builtin_amdgcn_exp2f(asf(m0[0]) - M);
    e *= __builtin_amdgcn_rcpf(xrow_sum(row_sum(e * asf(m0[2]))));
    TIE(e);
    WAITV1(0, p0);
    SETTLE(rws, oo, T, true, p0, 0, POL_LATE);
    float v = fold16(fold32(e * blo(p0[0]), e * blo(p0[2])), fold32(e * bhi(p0[0]), e * bhi(p0[2])));
    v += dpp<ROR4>(v);
    v += dpp<ROR8>(v);
    (&L.um[0][0])[slot] = cvtpk(v, v);
    if ((b >> 3) < 18) BAR();
    return;
  }
  const int rot = s + h;
#define KVS(j) ((((j) + rot) & 15) << 12)
#define THR() WAITN(WND - 1)
  u32x4 ak[16], av[16];
  const int kvoff = w * 1024 + ((l & 15) >> 2) * 256 + (l & 3) * 16 + (l >> 4) * 64;
  const int vvoff = w * 1024 + l * 16;
  unsigned xq4; u32x2 xq; u32x3 x1; u32x4 a1[6];
  const int r6 = t / 36, c36 = t - r6 * 36;
  {
    const int g = l >> 3, xo = (144 * rg + 36 * w) * 2;
    const int o0 = xo + 8 * g, o1 = g < 4 ? xo + 64 + 2 * g : OOB;
    LD2Z(xq, rx, o0, ""); LDHZ(xq4, rx, o1, "");
    const int v1 = t < 216 ? r6 * 6 * 9216 + c36 * 16 : OOB, x1o = (36 * s + 6 * min(r6, 5)) * 2;
    LD3Z(x1, rx, x1o, "");
#pragma unroll
    for (int j = 0; j < KA; ++j) LD4(ak[j], rk, kvoff, KVS(j), "nt");
#pragma unroll
    for (int j = 0; j < 6; ++j) LD4(a1[j], rw1, v1, j * 9216, "nt");
  }
#pragma unroll
  for (int j = KA; j < K0; ++j) LD4(ak[j], rk, kvoff, KVS(j), "nt");
  u32x4 msk;
  {
    const unsigned half = (l & 1) ? 0xffff0000u : 0x0000ffffu;
#pragma unroll
    for (int k = 0; k < 4; ++k) { msk[k] = (((l & 7) >> 1) == k) ? half : 0u; TIE(msk[k]); }
  }
  PIN();
  asm volatile("s_waitcnt lgkmcnt(0)"); PIN(); asm volatile("" : "+s"(epoch));
  const unsigned T = epoch + 1u ? epoch + 1u : 1u;
  WAITN(7 + K0); TIE(xq); TIE(xq4); TIE(aq[0]); TIE(aq[1]); TIE(aq[2]); TIE(aq[3]); TIE(aq[4]);
  {
    f32x4 acc = {0.f, 0.f, 0.f, 0.f};
    acc = rowmix(__builtin_amdgcn_perm(xq[0], xq[0], 0x01000100u), msk, aq[0], acc);
    acc = rowmix(__builtin_amdgcn_perm(xq[0], xq[0], 0x03020302u), msk, aq[1], acc);
    acc = rowmix(__builtin_amdgcn_perm(xq[1], xq[1], 0x01000100u), msk, aq[2], acc);
    acc = rowmix(__builtin_amdgcn_perm(xq[1], xq[1], 0x03020302u), msk, aq[3], acc);
    acc = rowmix(__builtin_amdgcn_perm(xq4, xq4, 0x01000100u), msk, aq[4], acc);
    if ((l >> 5) == ((l >> 3) & 1)) *(f32x4*)&L.qred[2 * w + (l >> 5)][8 * (l & 7) + 4 * ((l >> 4) & 1)] = acc;
  }
  BAR();
  PIN();
#pragma unroll
  for (int j = K0; j < K1; ++j) LD4(ak[j], rk, kvoff, KVS(j), "nt");
  PIN();
  {
    float acc[8] = {0.f, 0.f, 0.f, 0.f, 0.f, 0.f, 0.f, 0.f};
    WAITN(K1 - KA); TIE(x1);
#pragma unroll
    for (int j = 0; j < 6; ++j) TIE(a1[j]);
#pragma unroll
    for (int j = 0; j < 6; ++j) fma8(acc, a1[j], (j & 1) ? bhi(x1[j >> 1]) : blo(x1[j >> 1]));
    if (t < 216) {
      *(f32x4*)&L.hb[r6][8 * c36] = f32x4{acc[0], acc[1], acc[2], acc[3]};
      *(f32x4*)&L.hb[r6][8 * c36 + 4] = f32x4{acc[4], acc[5], acc[6], acc[7]};
    }
  }
  BAR();
#pragma unroll
  for (int j = K1; j < 16; ++j) { THR(); LD4(ak[j], rk, kvoff, KVS(j), "nt"); }
  unsigned b1v, b2v;
  const int fin = (b & 7) * 18 + (b >> 3);
  const int ocol = (fin < 128 ? (fin >> 5) * 128 + (fin & 15) * 8 + ((fin >> 4) & 1) * 4 : 512 + (fin & 7) * 8 + ((fin >> 3) & 1) * 4) + (t & 3);
  {
    const int bo = t >= 64 ? (((t - 64) >> 4) * 4 + (t & 3)) * 2 : OOB, b2o = (b >> 3) < 18 ? ocol * 2 : OOB;
    LDHZ(b1v, rb1, bo, ""); LDHZ(b2v, rb2, b2o, "");
  }
  const int qo = WS_Q + (h * 16 + (l >> 4) * 4 + w) * 512 + (l & 15) * 16, qo2 = qo + 256;
  u32x4 pa, pb, pc, pd;
#pragma unroll
  for (int j = 0; j < QP; ++j) { THR(); LD4(av[j], rv, vvoff, KVS(j), "nt"); }
  LD4Z(pa, rws, qo, POL_EARLY); LD4Z(pb, rws, qo2, POL_EARLY);
  PIN();
  u32x4 a2[5], ao[4], a2x[3], aox[2];
  const int rho = l >> 4;
  const int wvo = rho * 1152 + w * 256 + (l & 15) * 16;
  int x2o_ = w == 3 ? (l >> 3) * 1152 + 1024 + (l & 7) * 16 : OOB;
  constexpr int NPRE = N4 + N5;
#define LDW(i) do { \
    if ((i) < 5) LD4(a2[(i) < 5 ? (i) : 0], rw2, wvo, (i) * 4608, "nt"); \
    else if ((i) < 8) LD4(a2x[(i) >= 5 && (i) < 8 ? (i) - 5 : 0], rw2, x2o_, ((i) - 5) * 9216, "nt"); \
    else if ((i) < 12) LD4(ao[(i) >= 8 && (i) < 12 ? (i) - 8 : 0], rwo, wvo, ((i) - 8) * 4608, "nt"); \
    else if ((i) < 14) LD4(aox[(i) >= 12 && (i) < 14 ? (i) - 12 : 0], rwo, x2o_, ((i) - 12) * 9216, "nt"); } while (0)
#define LDWS(a, n) do { if ((n) > 0) LDW(a); if ((n) > 1) LDW((a) + 1); if ((n) > 2) LDW((a) + 2); if ((n) > 3) LDW((a) + 3); PIN(); } while (0)
  const int th_ = t - 64;
  const bool hl = th_ >= 0 && th_ < 80;
  int ho = hl ? WS_H + (hc * 16 + (t & 15)) * H_ROW + (s * 5 + (th_ >> 4)) * 16 : OOB;
  TIE(ho);
  int opo = WS_O + gid * O_ROW + l * 16;
  int obw = (w * 256 + (l >> 5) * 128 + (l & 15) * 8 + ((l >> 4) & 1) * 4) * 4;
  const int row = 4 * (th_ >> 4) + (l & 3);
  int slot = (l & 4) ? 48 + (row & 7) * 6 + (row >> 3) + 2 : (row & 3) * 12 + (row >> 2) + 4;
  slot = (l & 8) == 0 && hl && row < 18 ? slot : 10;
  int yo = l < 32 ? WS_Y + b * Y_ROW + (32 * w + l) * 16 : OOB;
  int yxo = (l & 40) == 0 ? WS_Y + b * Y_ROW + (128 + 8 * rho + (l & 7)) * 16 : OOB;
  int fo = WS_Y + t * Y_ROW + fin * 16, oc2 = ocol * 2;
  TIE(opo); TIE(obw); TIE(slot); TIE(yo); TIE(yxo); TIE(fo); TIE(oc2);
  TIE(x2o_);
  unsigned xe = (l & 4) ? 0u : ~0u, me[2], mo[2];
#pragma unroll
  for (int k = 0; k < 2; ++k) {
    const unsigned m = ((l & 3) >> 1) == k ? ((l & 1) ? 0xffff0000u : 0x0000ffffu) : 0u;
    me[k] = m & xe; mo[k] = m & ~xe; TIE(me[k]); TIE(mo[k]);
  }
  TIE(xe);
  PIN();
#pragma unroll
  for (int j = QP; j < QB; ++j) { THR(); LD4(av[j], rv, vvoff, KVS(j), "nt"); }
  LD4Z(pc, rws, qo, POL_EARLY); LD4Z(pd, rws, qo2, POL_EARLY);
#pragma unroll
  for (int j = QB; j < 16; ++j) { THR(); LD4(av[j], rv, vvoff, KVS(j), "nt"); }
  PIN();
  WAITN(16 - QP + 2); TIE(pa); TIE(pb); TIE(b1v); TIE(b2v);
#pragma unroll
  for (int j = 0; j < 16; ++j) TIE(ak[j]);
  if (BALLOT(!(pair_ok(pa, T) & pair_ok(pb, T))) != 0) {
    WAITN(16 - QB); TIE(pc); TIE(pd);
    pa = pc; pb = pd;
    SETTLE(rws, qo, T, true, pa, 0, POL_EARLY);
    SETTLE(rws, qo2, T, true, pb, 0, POL_EARLY);
  }
  {
    const float v = fold16(fold32(asf(pa[0]), asf(pb[0])), fold32(asf(pa[2]), asf(pb[2])));
    L.qs[64 * w + 32 * (l >> 5) + 2 * (l & 15) + ((l >> 4) & 1)] = (u16)cvtpk(v, v);
  }
  BAR();
  u32x4 ph;
  LD4Z(ph, rws, ho, POL_EARLY);
  PIN();
  const u32x4 qf = *(const u32x4*)&L.qs[((l >> 3) & 1) * 128 + ((l & 3) + 4 * (l >> 4)) * 8];
  constexpr float C2 = 0.08838834764831845f * 1.4426950408889634f;
  float sc[8];
  {
    const s16x8 qe = __builtin_bit_cast(s16x8, qf & xe), qo_ = __builtin_bit_cast(s16x8, qf & ~xe);
    f32x4 d[8];
#define QK(i) { d[i] = __builtin_amdgcn_mfma_f32_16x16x32_bf16(__builtin_bit_cast(s16x8, ak[2 * (i) + 1]), qo_, \
                       __builtin_amdgcn_mfma_f32_16x16x32_bf16(__builtin_bit_cast(s16x8, ak[2 * (i)]), qe, f32x4{0.f, 0.f, 0.f, 0.f}, 0, 0, 0), 0, 0, 0); PIN(); }
#define DIAG(j) { asm volatile("v_mov_b32_dpp %0, %1 quad_perm:[0,0,0,0] row_mask:0xf bank_mask:0xf\n\t" \
                               "v_add_f32_dpp %0, %2, %0 quad_perm:[1,1,1,1] row_mask:0xf bank_mask:0xf\n\t" \
                               "v_add_f32_dpp %0, %3, %0 quad_perm:[2,2,2,2] row_mask:0xf bank_mask:0xf\n\t" \
                               "v_add_f32_dpp %0, %4, %0 quad_perm:[3,3,3,3] row_mask:0xf bank_mask:0xf" \
                               : "=&v"(sc[j]) : "v"(d[j][0]), "v"(d[j][1]), "v"(d[j][2]), "v"(d[j][3])); PIN(); }
    QK(0) QK(1) QK(2)
    asm volatile("s_nop 4");
    PIN();
    DIAG(0) QK(3) DIAG(1) QK(4) DIAG(2) QK(5) DIAG(3) QK(6) DIAG(4) QK(7) DIAG(5) DIAG(6)
    asm volatile("s_nop 1");
    PIN();
    DIAG(7)
  }
  PIN();
  LDWS(0, N4);
  float mx = -INFINITY;
#pragma unroll
  for (int j = 0; j < 8; ++j) mx = fmaxf(mx, sc[j]);
  mx = xrow_max(fmaxf(mx, dpp<HMIR>(mx))) * C2;
  f32x2 ls2 = {0.f, 0.f};
  unsigned pk[8];
#pragma unroll
  for (int j = 0; j < 8; j += 2) {
    const f32x2 p = {__builtin_amdgcn_exp2f(fmaf(sc[j], C2, -mx)), __builtin_amdgcn_exp2f(fmaf(sc[j + 1], C2, -mx))};
    ls2 += p; pk[j] = cvtpk(p[0], p[0]); pk[j + 1] = cvtpk(p[1], p[1]);
  }
  float lsum = ls2[0] + ls2[1];
  lsum = xrow_sum(lsum + dpp<HMIR>(lsum));
  if ((l & 55) == 0) *(f32x2*)&L.ml[(l >> 3) * 4 + w][0] = f32x2{mx, lsum};
  PIN();
  LDWS(N4, N5);
  float gate = L.gt[0], ngate = L.gt[1];
  TIE(gate); TIE(ngate);
  f32x4 olo = {0.f, 0.f, 0.f, 0.f}, ohi = {0.f, 0.f, 0.f, 0.f};
#define NW(j) (NPRE + (j) - WD < NPRE ? NPRE : NPRE + (j) - WD > 14 ? 14 : NPRE + (j) - WD)
#define PV_STEP(j) { WAITN(15 - (j) + 1 + NW(j)); TIE(av[j]); \
    const s16x4 a_ = __builtin_bit_cast(s16x4, u32x2{pk[(j) >> 1] & ((j) & 1 ? mo[0] : me[0]), pk[(j) >> 1] & ((j) & 1 ? mo[1] : me[1])}); \
    olo = __builtin_amdgcn_mfma_f32_16x16x16bf16_1k(a_, __builtin_bit_cast(s16x4, u32x2{av[j][0], av[j][1]}), olo, 0, 0, 0); \
    ohi = __builtin_amdgcn_mfma_f32_16x16x16bf16_1k(a_, __builtin_bit_cast(s16x4, u32x2{av[j][2], av[j][3]}), ohi, 0, 0, 0); \
    PIN(); if ((j) >= WD) LDW(NW(j)); }
  PV_STEP(0) PV_STEP(1) PV_STEP(2) PV_STEP(3)
  BAR();
  float osc;
  {
    const f32x2 v = *(const f32x2*)&L.ml[l & 7][0];
    float M = fmaxf(v[0], dpp<XOR1>(v[0]));
    M = fmaxf(M, dpp<XOR2>(M));
    const float e = __builtin_amdgcn_exp2f(v[0] - M);
    const float c0_ = asf(__builtin_amdgcn_readlane(asu(e), w)), c1_ = asf(__builtin_amdgcn_readlane(asu(e), 4 + w));
    osc = l < 32 ? c0_ : c1_;
    TIE(osc);
  }
  PIN();
  PV_STEP(4) PV_STEP(5) PV_STEP(6) PV_STEP(7) PV_STEP(8) PV_STEP(9) PV_STEP(10) PV_STEP(11) PV_STEP(12) PV_STEP(13)
  TIE(pc); TIE(pd);
  PV_STEP(14) PV_STEP(15)
  LDWS(NW(16), 14 - NW(16));
  *(f32x4*)((char*)&L.ob[0][0] + obw) = f32x4{fold16(olo[0], ohi[0]), fold16(olo[1], ohi[1]), fold16(olo[2], ohi[2]), fold16(olo[3], ohi[3])} * osc;
  BAR();
  if (w == 3) {
    const f32x4 v = (*(const f32x4*)&L.ob[0][4 * l] + *(const f32x4*)&L.ob[1][4 * l]) + (*(const f32x4*)&L.ob[2][4 * l] + *(const f32x4*)&L.ob[3][4 * l]);
    PUT_QUAD(rws, opo, T, v[0], v[1], v[2], v[3], POL_LATE);
  }
  PIN();
  {
    WAITN(14);
    TIE(ph);
    if (w == 1 || w == 2) {
      u32x4 p0 = ph;
      SETTLE(rws, ho, T, hl, p0, 0, POL_EARLY);
      const float s0 = row_sum(blo(p0[0])), s1 = row_sum(bhi(p0[0])), s2 = row_sum(blo(p0[2])), s3 = row_sum(bhi(p0[2]));
      const float sv = (t & 2) ? ((t & 1) ? s3 : s2) : ((t & 1) ? s1 : s0);
      const float hv = rbf(rbf(sv) + blo(b1v));
      const float z = 0.7978845608028654f * (hv + 0.044715f * hv * hv * hv);
      const float th = 1.f - 2.f * __builtin_amdgcn_rcpf(1.f + __expf(2.f * z));
      const float v = 0.5f * hv * (1.f + th);
      (&L.um[0][0])[slot] = cvtpk(v, v);
      arrive(&L.sync[S_ACT]);
    }
    await<0>(&L.sync[S_ACT], 2);
    if (w == 3) WAITN(7); else WAITN(6);
#pragma unroll
    for (int j = 0; j < 5; ++j) TIE(a2[j]);
    TIE(a2x[0]); TIE(a2x[1]); TIE(a2x[2]);
    const f32x4 zero = {0.f, 0.f, 0.f, 0.f};
    const unsigned* ux = &L.ux[l >> 3][0];
    const u32x4 ua = *(const u32x4*)&L.um[rho][4];
    const unsigned ua4 = L.um[rho][8];
    f32x4 yb = rowmix(ua4, msk, a2[4], rowmix(ua[3], msk, a2[3], rowmix(ua[2], msk, a2[2], rowmix(ua[1], msk, a2[1], rowmix(ua[0], msk, a2[0], zero))))) * ngate;
    f32x4 xb = zero;
    if (w == 3) xb = rowmix(ux[4], msk, a2x[2], rowmix(ux[3], msk, a2x[1], rowmix(ux[2], msk, a2x[0], zero))) * ngate;
    TIE(yb); TIE(xb);
    PIN();
    if (w == 3) WAITN(1); else WAITN(0);
#pragma unroll
    for (int j = 0; j < 4; ++j) TIE(ao[j]);
    TIE(aox[0]); TIE(aox[1]);
    u32x4 uo; u32x2 uox;
    {
      const unsigned a0 = (unsigned)(size_t)(lds_u32*)&L.um[rho][0], a1_ = (unsigned)(size_t)(lds_u32*)ux;
#pragma unroll 1
      for (unsigned spins = 0; spins < (1u << 22); ++spins) {
        asm volatile("ds_read_b128 %0, %2\n\tds_read_b64 %1, %3\n\ts_waitcnt lgkmcnt(0)" : "=&v"(uo), "=&v"(uox) : "v"(a0), "v"(a1_) : "memory");
        if (BALLOT(max(max(max(uo[0], uo[1]), max(uo[2], uo[3])), max(uox[0], uox[1])) == NOTYET) == 0) break;
      }
    }
    const f32x4 ya = rowmix(uo[1], msk, ao[1], rowmix(uo[0], msk, ao[0], zero)) + rowmix(uo[3], msk, ao[3], rowmix(uo[2], msk, ao[2], zero));
    const f32x4 y = gate * ya + yb;
    PUT_QUAD(rws, yo, T, y[0], y[1], y[2], y[3], POL_LATE);
    if (w == 3) {
      f32x4 z = gate * (rowmix(uox[0], msk, aox[0], zero) + rowmix(uox[1], msk, aox[1], zero)) + xb;
      const float z0 = fold32(rot8_hi(z[0]), rot8_hi(z[2])), z1 = fold32(rot8_hi(z[1]), rot8_hi(z[3]));
      const unsigned p = cvtpk(z0, z1);
      const auto q = __builtin_amdgcn_permlane32_swap(p, p, false, false);
      PUT_PAIR(rws, yxo, T, asf(q[0]), asf(q[1]), POL_LATE);
    }
  }
  PIN();
  if ((b >> 3) < 18) {
    float bias = ngate * blo(b2v);
    int fslot = (l & 15) == 0 ? rho * 4 + w : 16;
    TIE(bias); TIE(fslot);
    __builtin_amdgcn_s_sleep(FIN_SLEEP);
    LD4Z(pa, rws, fo, POL_LATE);
    WAITV1(0, pa);
    SETTLE(rws, fo, T, true, pa, FAR_SLEEP, POL_LATE);
    const float v = row_sum(fold16(fold32(blo(pa[0]), blo(pa[2])), fold32(bhi(pa[0]), bhi(pa[2]))));
    (&L.fin[0][0])[fslot] = v;
    BAR();
    if (t < 4) {
      const f32x4 f = *(const f32x4*)&L.fin[t][0];
      const float r = (f[0] + f[1]) + (f[2] + (f[3] + bias));
      const unsigned pr = cvtpk(r, r);
      STHZ(pr, rout, oc2, "");
    }
    if (b == 0 && t == 0) { const int z = WS_EPOCH; const unsigned tv = T; ST1Z(tv, rws, z, ""); }
  }
}

extern "C" void kernel_launch(void* const* d_in, const int* in_sizes, int n_in, void* d_out, int out_size, void* d_ws, size_t ws_size, hipStream_t stream) {
  fused_decode<<<256, 320, 0, stream>>>((const u16*)d_in[3], (const u16*)d_in[0], (unsigned char*)d_ws, (const u16*)d_in[6], (const u16*)d_in[1],
                                        (const u16*)d_in[10], (const u16*)d_in[2], (const u16*)d_in[8], (const u16*)d_in[4], (const u16*)d_in[7],
                                        (const u16*)d_in[9], (u16*)d_out);
}
```
